```python
import torch

def _softmax(x, axis=-1):
    return torch.softmax(x, dim=axis)
def _gelu(x):
    return torch.nn.functional.gelu(x, approximate="tanh")
def _silu(x):
    return torch.nn.functional.silu(x)
def _sigmoid(x):
    return torch.sigmoid(x)
def _layernorm(x, axis=-1, eps=1e-5):
    m = x.mean(dim=axis, keepdim=True)
    v = x.var(dim=axis, keepdim=True, unbiased=False)
    return (x - m) / torch.sqrt(v + eps)
def _scatter_add(tgt, idx, src, axis=0):
    return tgt.clone().index_add_(axis, idx, src)
def _segment_sum(src, seg, n):
    out = torch.zeros((n,) + src.shape[1:], dtype=src.dtype)
    return out.index_add_(0, seg, src)
def _pad(x, pw):
    flat = tuple(v for pair in reversed(pw) for v in pair)
    return torch.nn.functional.pad(x, flat)

kernel_name = 'recurrent_scan__sliding_local_dilated__glu_gated_reglu__prompt'

def setup_inputs():

    _w = (lambda a: (lambda t: t.to(torch.bfloat16) if t.is_floating_point() else t)(torch.as_tensor(a)))
    def _A_setup():
        B, S, D = 1, 2048, 512
        return {
            "x": _w((lambda *s: torch.randn(*s, dtype=torch.bfloat16) * 0.05)(B, S, D)),
            "wqkv": _w((lambda *s: torch.randn(*s, dtype=torch.bfloat16) * 0.2)(D, 3 * D)),
            "wo": _w((lambda *s: torch.randn(*s, dtype=torch.bfloat16) * 0.2)(D, D)),
        }
    _A_in = _A_setup()

    def _B_setup():
        B, S, D, F = 1, 2048, 512, 1792
        return {
            "x": _w((lambda *s: torch.randn(*s, dtype=torch.bfloat16) * 0.05)(B, S, D)),
            "W_gate": _w((lambda *s: torch.randn(*s, dtype=torch.bfloat16) * 0.1)(D, F) * (2.0 / D) ** 0.5),
            "W_up": _w((lambda *s: torch.randn(*s, dtype=torch.bfloat16) * 0.1)(D, F) * (2.0 / D) ** 0.5),
            "W_down": _w((lambda *s: torch.randn(*s, dtype=torch.bfloat16) * 0.1)(F, D) * (1.0 / F) ** 0.5),
        }
    _B_in = _B_setup()

    return {
        "A_x": _A_in["x"],
        "A_wqkv": _A_in["wqkv"],
        "A_wo": _A_in["wo"],
        "B_x": _B_in["x"],
        "B_W_gate": _B_in["W_gate"],
        "B_W_up": _B_in["W_up"],
        "B_W_down": _B_in["W_down"],
    }

def reference(A_x, A_wqkv, A_wo, B_x, B_W_gate, B_W_up, B_W_down):
    def _A_ref(x, wqkv, wo):
        B, S, D = x.shape
        H = 32; Dh = D // H
        W, dil = 8, 2
        qkv = (lambda x, *d: x.permute(*d))((x @ wqkv).reshape(B, S, 3, H, Dh), 2, 0, 3, 1, 4)
        q, k, v = qkv[0], qkv[1], qkv[2]
        scale = Dh ** -0.5
        pos = torch.arange(S)
        off = torch.arange(W) * dil
        idx = pos[:, None] - off[None, :]
        mask = idx >= 0
        idx_c = torch.where(mask, idx, 0).reshape(-1)
        kg = k[:, :, idx_c, :].reshape(B, H, S, W, Dh)
        vg = v[:, :, idx_c, :].reshape(B, H, S, W, Dh)
        logits = torch.einsum("bhsd,bhswd->bhsw", q, kg) * scale
        logits = torch.where(mask[None, None, :, :], logits, -1e9)
        attn = _softmax(logits, axis=-1)
        o = torch.einsum("bhsw,bhswd->bhsd", attn, vg)
        return (lambda x, *d: x.permute(*d))(o, 0, 2, 1, 3).reshape(B, S, D) @ wo

    def _B_ref(x, W_gate, W_up, W_down):
        gate = torch.einsum("bsd,df->bsf", x, W_gate)
        up = torch.einsum("bsd,df->bsf", x, W_up)
        gate = gate * (lambda x, dt: x.to(dt))(gate > 0, gate.dtype)
        h = gate * up
        return torch.einsum("bsf,fd->bsd", h, W_down)

    x = A_x
    xn = _layernorm(x, axis=-1)
    s = _A_ref(xn, A_wqkv, A_wo)
    g = _silu(_B_ref(xn, B_W_gate, B_W_up, B_W_down))
    return x + s * g
```

```cpp
#include <hip/hip_runtime.h>
#include <hip/hip_bf16.h>
typedef unsigned short u16;
__device__ __forceinline__ float bf2f(u16 v){ return __uint_as_float(((unsigned)v)<<16); }
__device__ __forceinline__ u16 f2bf(float f){ unsigned u=__float_as_uint(f); u += 0x7FFF + ((u>>16)&1); return (u16)(u>>16); }
__device__ __forceinline__ float rbf(float f){ return bf2f(f2bf(f)); }

__global__ void ln_k(const u16* x, u16* xn){
  int row=blockIdx.x; int t=threadIdx.x;
  float v[8]; float s=0;
  for(int i=0;i<8;i++){ v[i]=bf2f(x[row*512+t*8+i]); s+=v[i]; }
  for(int o=32;o;o>>=1) s+=__shfl_xor(s,o);
  float m=s/512.f; float q=0;
  for(int i=0;i<8;i++){ float d=v[i]-m; q+=d*d; }
  for(int o=32;o;o>>=1) q+=__shfl_xor(q,o);
  float var=rbf(q/512.f); float mb=rbf(m);
  float sd=rbf(sqrtf(rbf(var+1e-5f)));
  for(int i=0;i<8;i++) xn[row*512+t*8+i]=f2bf(rbf(v[i]-mb)/sd);
}
__global__ void gemm_k(const u16* A,const u16* B,u16* C,int M,int N,int K){
  __shared__ float As[16][17], Bs[16][17];
  int tx=threadIdx.x, ty=threadIdx.y; int row=blockIdx.y*16+ty, col=blockIdx.x*16+tx;
  float acc=0;
  for(int k0=0;k0<K;k0+=16){
    As[ty][tx]=bf2f(A[row*K+k0+tx]); Bs[ty][tx]=bf2f(B[(k0+ty)*N+col]);
    __syncthreads();
    for(int k=0;k<16;k++) acc+=As[ty][k]*Bs[k][tx];
    __syncthreads();
  }
  C[row*N+col]=f2bf(acc);
}
__global__ void attn_k(const u16* qkv,u16* o){
  int idx=blockIdx.x*blockDim.x+threadIdx.x;
  int s=idx>>5, h=idx&31;
  float q[16]; for(int d=0;d<16;d++) q[d]=bf2f(qkv[s*1536+h*16+d]);
  float lg[8]; float mx=-3e38f;
  for(int w=0;w<8;w++){ int p=s-2*w; if(p>=0){ float a=0; for(int d=0;d<16;d++) a+=q[d]*bf2f(qkv[p*1536+512+h*16+d]); lg[w]=rbf(rbf(a)*0.25f);} else lg[w]=rbf(-1e9f); mx=fmaxf(mx,lg[w]); }
  float sum=0; for(int w=0;w<8;w++){ lg[w]=__expf(lg[w]-mx); sum+=lg[w]; }
  float acc[16]; for(int d=0;d<16;d++) acc[d]=0;
  for(int w=0;w<8;w++){ int p=s-2*w; if(p<0)p=0; float pw=rbf(lg[w]/sum); for(int d=0;d<16;d++) acc[d]+=pw*bf2f(qkv[p*1536+1024+h*16+d]); }
  for(int d=0;d<16;d++) o[s*512+h*16+d]=f2bf(acc[d]);
}
__global__ void reglu_k(const u16* g,const u16* u,u16* h,int n){
  int i=blockIdx.x*blockDim.x+threadIdx.x; if(i<n){ float a=bf2f(g[i]); a=a>0?a:0.f; h[i]=f2bf(a*bf2f(u[i])); }
}
__global__ void fin_k(const u16* x,const u16* s,const u16* dn,u16* out,int n){
  int i=blockIdx.x*blockDim.x+threadIdx.x; if(i<n){ float d=bf2f(dn[i]); float g=rbf(d/(1.f+__expf(-d))); out[i]=f2bf(bf2f(x[i])+rbf(bf2f(s[i])*g)); }
}
extern "C" void kernel_launch(void* const* d_in, const int* in_sizes, int n_in, void* d_out, int out_size, void* d_ws, size_t ws_size, hipStream_t stream){
  const u16* x=(const u16*)d_in[0]; const u16* wqkv=(const u16*)d_in[1]; const u16* wo=(const u16*)d_in[2];
  const u16* wg=(const u16*)d_in[4]; const u16* wu=(const u16*)d_in[5]; const u16* wd=(const u16*)d_in[6];
  u16* ws=(u16*)d_ws;
  u16* xn=ws; u16* qkv=xn+2048*512; u16* o=qkv+2048*1536; u16* s=o+2048*512; u16* g=s+2048*512; u16* u=g+2048*1792; u16* h=u+2048*1792; u16* dn=h+2048*1792;
  ln_k<<<2048,64,0,stream>>>(x,xn);
  gemm_k<<<dim3(1536/16,128),dim3(16,16),0,stream>>>(xn,wqkv,qkv,2048,1536,512);
  attn_k<<<2048*32/256,256,0,stream>>>(qkv,o);
  gemm_k<<<dim3(512/16,128),dim3(16,16),0,stream>>>(o,wo,s,2048,512,512);
  gemm_k<<<dim3(1792/16,128),dim3(16,16),0,stream>>>(xn,wg,g,2048,1792,512);
  gemm_k<<<dim3(1792/16,128),dim3(16,16),0,stream>>>(xn,wu,u,2048,1792,512);
  reglu_k<<<2048*1792/256,256,0,stream>>>(g,u,h,2048*1792);
  gemm_k<<<dim3(512/16,128),dim3(16,16),0,stream>>>(h,wd,dn,2048,512,1792);
  fin_k<<<2048*512/256,256,0,stream>>>(x,s,dn,(u16*)d_out,2048*512);
}
```

```cpp
#include <hip/hip_runtime.h>
#include <utility>
typedef unsigned short u16;
typedef __attribute__((ext_vector_type(4))) short s16x4;
typedef __attribute__((ext_vector_type(8))) short s16x8;
typedef __attribute__((ext_vector_type(4))) float f32x4;
typedef __attribute__((ext_vector_type(2))) float f32x2;
typedef __attribute__((ext_vector_type(2))) __bf16 bf16x2;
typedef __attribute__((ext_vector_type(8))) __bf16 bf16x8;
typedef __attribute__((ext_vector_type(2))) unsigned u32x2;
typedef __attribute__((ext_vector_type(4))) unsigned u32x4;
#define LAS __attribute__((address_space(3)))
#define DI __device__ __forceinline__

template <int... I, class F> DI void static_for_impl(std::integer_sequence<int, I...>, F&& f) { (f(std::integral_constant<int, I>{}), ...); }
template <int N, class F> DI void static_for(F&& f) { static_for_impl(std::make_integer_sequence<int, N>{}, f); }

template <int N> DI void wait_vm() { asm volatile("s_waitcnt vmcnt(%0)" ::"i"(N) : "memory"); }
DI void wait_lgkm0() { asm volatile("s_waitcnt lgkmcnt(0)" ::: "memory"); }
DI void bar() { __builtin_amdgcn_s_barrier(); }
DI void dma16(unsigned lds_dst, unsigned voff, const void* sbase) {
  unsigned keep;
  asm volatile("s_mov_b32 %0, m0\n\ts_mov_b32 m0, %1\n\ts_nop 0\n\tglobal_load_lds_dwordx4 %2, %3\n\ts_mov_b32 m0, %0"
               : "=&s"(keep) : "s"(lds_dst), "v"(voff), "s"(sbase) : "memory");
}
DI f32x4 mfma32(u32x4 a, u32x4 b, f32x4 c) {
  return __builtin_amdgcn_mfma_f32_16x16x32_bf16(__builtin_bit_cast(bf16x8, a), __builtin_bit_cast(bf16x8, b), c, 0, 0, 0);
}
DI f32x4 mfma16(u32x2 a, u32x2 b, f32x4 c) {
  return __builtin_amdgcn_mfma_f32_16x16x16bf16_1k(__builtin_bit_cast(s16x4, a), __builtin_bit_cast(s16x4, b), c, 0, 0, 0);
}
DI float dot2(unsigned a, unsigned b, float c) {
  return __builtin_amdgcn_fdot2_f32_bf16(__builtin_bit_cast(bf16x2, a), __builtin_bit_cast(bf16x2, b), c, false);
}
DI unsigned pk2(float lo, float hi) { bf16x2 c; c[0] = (__bf16)lo; c[1] = (__bf16)hi; return __builtin_bit_cast(unsigned, c); }
DI float lo2f(unsigned u) { return __uint_as_float(u << 16); }
DI float hi2f(unsigned u) { return __uint_as_float(u & 0xffff0000u); }
DI float rbf(float f) { return lo2f(pk2(f, f)); }
DI u32x2 pk4(f32x4 v) { u32x2 r; r[0] = pk2(v[0], v[1]); r[1] = pk2(v[2], v[3]); return r; }
DI float gsum(float v) {
  unsigned u = __float_as_uint(v);
  auto r = __builtin_amdgcn_permlane32_swap(u, u, false, false);
  v = __uint_as_float(r[0]) + __uint_as_float(r[1]);
  u = __float_as_uint(v);
  auto r2 = __builtin_amdgcn_permlane16_swap(u, u, false, false);
  return __uint_as_float(r2[0]) + __uint_as_float(r2[1]);
}
DI float gmax(float v) {
  unsigned u = __float_as_uint(v);
  auto r = __builtin_amdgcn_permlane32_swap(u, u, false, false);
  v = fmaxf(__uint_as_float(r[0]), __uint_as_float(r[1]));
  u = __float_as_uint(v);
  auto r2 = __builtin_amdgcn_permlane16_swap(u, u, false, false);
  return fmaxf(__uint_as_float(r2[0]), __uint_as_float(r2[1]));
}
DI u32x4 lds_b128(const char* lds, unsigned off) { return *(const u32x4*)(lds + off); }
DI u32x4 lds_tr2(const char* lds, unsigned off, unsigned step) {
  s16x4 t0 = __builtin_amdgcn_ds_read_tr16_b64_v4i16((LAS s16x4*)(lds + off));
  s16x4 t1 = __builtin_amdgcn_ds_read_tr16_b64_v4i16((LAS s16x4*)(lds + off + step));
  u32x2 a = __builtin_bit_cast(u32x2, t0), b = __builtin_bit_cast(u32x2, t1);
  u32x4 r; r[0] = a[0]; r[1] = a[1]; r[2] = b[0]; r[3] = b[1];
  return r;
}

constexpr int A_STAGE = 18432, B_STAGE = 20480;
constexpr int B_OFF = 0, A_OFF = 5 * B_STAGE;
constexpr int T_OFF = A_OFF + 3 * A_STAGE;
constexpr int ST_OFF = T_OFF;
constexpr int HP_OFF = ST_OFF + 1024;
constexpr int CT_OFF = HP_OFF + 1024;
constexpr int LDS1 = CT_OFF + 2560;
constexpr int NKS = 72;

DI void finish_stats(float s1, float s2, float& mb, float& inv) {
  float mean = s1 * (1.f / 512.f);
  float var = fmaxf(s2 * (1.f / 512.f) - mean * mean, 0.f);
  mb = rbf(mean);
  float sd = rbf(sqrtf(rbf(rbf(var) + 1e-5f)));
  inv = 1.f / sd;
}

template <bool ISQ>
DI void phase1(const u16* __restrict__ x, const u16* __restrict__ wqkv, const u16* __restrict__ wg, const u16* __restrict__ wu,
               u16* __restrict__ a2, char* lds, int mt, int j) {
  const int tid = threadIdx.x, lane = tid & 63;
  const int w = __builtin_amdgcn_readfirstlane(tid >> 6), wm = w >> 2, wn = w & 3;
  const int li = lane & 15, g = lane >> 4;
  const unsigned lbase = (unsigned)(size_t)lds;
  int fa = 0, fx = 0;
  if (!ISQ) { int i = j - 8, f0 = 512 + 160 * i; if (i & 1) { fx = f0; fa = f0 + 32; } else { fa = f0; fx = f0 + 128; } }
  const char* sb0; const char* sb1;
  unsigned vo0, vo1;
  unsigned ld = 0;
  const bool bw = w < 5;
  const bool mixed = (!ISQ) && (w == 4);
  if (bw) {
    if (ISQ) { ld = w < 3 ? 3072 : 3584; sb0 = (w < 3) ? (const char*)(wqkv + w * 512 + 64 * j) : (const char*)((w == 3 ? wg : wu) + 64 * j); }
    else { ld = 3584; sb0 = (w < 4) ? (const char*)((w < 2 ? wg : wu) + fa + 64 * (w & 1)) : (const char*)(wg + fx); }
    sb1 = mixed ? (const char*)(wu + fx) : sb0;
    if (!mixed) {
      int q = lane >> 3, slot = lane & 7, jp = slot >> 1, e = slot & 1;
      int s0 = (q >> 1) & 1, s1 = s0 | 2;
      vo0 = q * ld + 16 * (2 * (jp ^ s0) + e);
      vo1 = (q + 8) * ld + 16 * (2 * (jp ^ s1) + e);
    } else {
      int r16 = lane >> 2, cp = lane & 3, s0 = (r16 >> 3) & 1;
      vo0 = r16 * ld + 16 * (cp ^ s0);
      vo1 = r16 * ld + 16 * (cp ^ (s0 | 2));
    }
  } else {
    int lrow = lane >> 3, slot = lane & 7;
    vo0 = lrow * 2048 + 16 * (slot ^ (lrow >> 1));
    vo1 = lrow * 2048 + 16 * (slot ^ (4 + (lrow >> 1)));
    sb0 = (const char*)x; sb1 = sb0;
  }
  const int T0 = mt * 128;
  auto issueB = [&](int t) {
    unsigned dst = lbase + B_OFF + (t % 5) * B_STAGE + w * 4096;
    size_t ko = (size_t)(32 * t) * ld, h16 = (size_t)16 * ld;
    dma16(dst, vo0, sb0 + ko);
    dma16(dst + 1024, vo1, sb1 + ko);
    dma16(dst + 2048, vo0, sb0 + ko + h16);
    dma16(dst + 3072, vo1, sb1 + ko + h16);
  };
  auto issueA = [&](int s, int half) {
    int a = w - 5;
#pragma unroll
    for (int i = 0; i < 3; ++i) {
      int pc = 6 * a + 3 * half + i;
      int tb = pc < 16 ? T0 + 32 * (pc >> 2) + 16 * (pc & 1) + ((pc >> 1) & 1) : (mt ? T0 - 16 : 0) + (pc & 1);
      unsigned dst = lbase + A_OFF + (s % 3) * A_STAGE + pc * 1024;
      dma16(dst, (pc & 1) ? vo1 : vo0, sb0 + (size_t)tb * 1024 + s * 128);
    }
  };
  if (bw) { issueB(0); issueB(1); issueB(2); issueB(3); issueB(4); }
  else { issueA(0, 0); issueA(0, 1); issueA(1, 0); issueA(1, 1); issueA(2, 0); issueA(2, 1); }

  unsigned aoff[2];
  { int rho = 64 * wm + li; int f = (rho >> 1) & 7; aoff[0] = A_OFF + rho * 128 + 16 * (g ^ f); aoff[1] = A_OFF + rho * 128 + 16 * ((4 + g) ^ f); }
  unsigned hoff[2];
  { int rho = wm ? (40 + (li & 7) + 16 * (li >> 3)) : 128 + li; int f = (rho >> 1) & 7; hoff[0] = A_OFF + rho * 128 + 16 * (g ^ f); hoff[1] = A_OFF + rho * 128 + 16 * ((4 + g) ^ f); }
  unsigned h2off[2];
  { int rho = 128 + li; int f = (rho >> 1) & 7; h2off[0] = A_OFF + rho * 128 + 16 * (g ^ f); h2off[1] = A_OFF + rho * 128 + 16 * ((4 + g) ^ f); }
  unsigned soff[2] = {aoff[0] + wn * 2048, aoff[1] + wn * 2048};
  unsigned boff[2][3];
  { int q = (lane >> 2) & 3, pp = lane & 3, s = ((q >> 1) & 1) | ((g & 1) << 1);
    unsigned rb = B_OFF + g * 1024 + 128 * q + 8 * pp;
    if (ISQ) {
      boff[0][0] = rb + 32 * (wn ^ s); boff[0][1] = boff[0][2] = 0;
    } else {
      int p0 = wn >> 1, j0 = 2 * (wn & 1);
      boff[0][0] = rb + p0 * 4096 + 32 * (j0 ^ s); boff[0][1] = rb + p0 * 4096 + 32 * ((j0 + 1) ^ s);
      int up = pp >> 1, d = 2 * (g >> 1) + up, r16 = 8 * (g & 1) + q, sg = (g & 1) | (up << 1);
      boff[0][2] = B_OFF + 16384 + d * 1024 + 64 * r16 + 16 * (wn ^ sg) + 8 * (pp & 1);
    }
#pragma unroll
    for (int c = 0; c < (ISQ ? 1 : 3); ++c) { boff[1][c] = boff[0][c] + 3 * B_STAGE; asm volatile("" : "+v"(boff[0][c]), "+v"(boff[1][c])); }
  }
  asm volatile("" : "+v"(aoff[0]), "+v"(aoff[1]), "+v"(soff[0]), "+v"(soff[1]));
  if (ISQ) asm volatile("" : "+v"(hoff[0]), "+v"(hoff[1]), "+v"(h2off[0]), "+v"(h2off[1]));
  f32x4 acc[4][5]; f32x4 acch[2];
#pragma unroll
  for (int r = 0; r < 4; ++r)
#pragma unroll
    for (int c = 0; c < 5; ++c) acc[r][c] = f32x4{0, 0, 0, 0};
  acch[0] = f32x4{0, 0, 0, 0}; acch[1] = f32x4{0, 0, 0, 0};
  float s1 = 0, s2 = 0, hs1 = 0, hs2 = 0, cs[5] = {0, 0, 0, 0, 0};
  const unsigned ONES = 0x3f803f80u;
  u32x4 af[2][4], ah[2], as_[2], bf[2][5];

  auto readfr = [&](auto tc) {
    constexpr int t = tc; constexpr int P = t & 1;
    constexpr int ao = ((t >> 1) % 3) * A_STAGE, bh_ = (t % 5) >= 3, bo = ((t % 5) - 3 * bh_) * B_STAGE;
#pragma unroll
    for (int c = 0; c < 5; ++c) {
      if (ISQ) bf[P][c] = lds_tr2(lds, boff[bh_][0] + bo + c * 4096, 512);
      else if (c < 4) bf[P][c] = lds_tr2(lds, boff[bh_][c & 1] + bo + (c >> 1) * 8192, 512);
      else bf[P][c] = lds_tr2(lds, boff[bh_][2] + bo, 256);
    }
#pragma unroll
    for (int r = 0; r < 4; ++r) af[P][r] = lds_b128(lds, aoff[P] + ao + r * 2048);
    if (ISQ) ah[P] = lds_b128(lds, hoff[P] + ao);
    as_[P] = lds_b128(lds, soff[P] + ao);
  };
  if (bw) wait_vm<16>(); else wait_vm<12>();
  bar();
  readfr(std::integral_constant<int, 0>{});

  static_for<16>([&](auto tc) {
    constexpr int t = tc; constexpr int P = t & 1;
    if constexpr (t < 15) {
      constexpr int lastB = (t + 4 < 15) ? t + 4 : 15;
      constexpr int nB = (lastB - (t + 1)) * 4;
      constexpr int issA = 18 + 3 * ((t - 1 < 0) ? 0 : (t - 1 > 10 ? 10 : t - 1));
      constexpr int nA = issA - 6 * (((t + 1) >> 1) + 1);
      if (bw) wait_vm<nB>(); else wait_vm<nA>();
      wait_lgkm0();
      bar();
      if (bw) { if constexpr (t + 5 < 16) issueB(t + 5); }
      else { if constexpr (t >= 1 && t <= 10) issueA((t - 1) / 2 + 3, (t - 1) & 1); }
      readfr(std::integral_constant<int, t + 1>{});
    }
#pragma unroll
    for (int r = 0; r < 4; ++r)
#pragma unroll
      for (int c = 0; c < 5; ++c) {
        if (ISQ && c == 2) acc[r][c] = mfma32(af[P][r], bf[P][c], acc[r][c]);
        else acc[r][c] = mfma32(bf[P][c], af[P][r], acc[r][c]);
      }
    if (ISQ) { acch[0] = mfma32(bf[P][1], ah[P], acch[0]); acch[1] = mfma32(ah[P], bf[P][2], acch[1]); }
#pragma unroll
    for (int e = 0; e < 4; ++e) { s1 = dot2(as_[P][e], ONES, s1); s2 = dot2(as_[P][e], as_[P][e], s2); }
    if (wm == P) {
#pragma unroll
      for (int c = 0; c < 5; ++c)
#pragma unroll
        for (int e = 0; e < 4; ++e) cs[c] = dot2(bf[P][c][e], ONES, cs[c]);
    }
    if (ISQ && w == (t >> 1)) {
      u32x4 hv = lds_b128(lds, h2off[P] + ((t >> 1) % 3) * A_STAGE);
#pragma unroll
      for (int e = 0; e < 4; ++e) { hs1 = dot2(hv[e], ONES, hs1); hs2 = dot2(hv[e], hv[e], hs2); }
    }
  });

  {
    float a = gsum(s1), b = gsum(s2), mb, inv;
    finish_stats(a, b, mb, inv);
    if (lane < 16) *(f32x2*)(lds + ST_OFF + (16 * (4 * wm + wn) + li) * 8) = f32x2{mb, inv};
    if (ISQ) { float ha = gsum(hs1), hb = gsum(hs2); if (lane < 16) *(f32x2*)(lds + HP_OFF + (w * 16 + li) * 8) = f32x2{ha, hb}; }
#pragma unroll
    for (int c = 0; c < 5; ++c) { float v = gsum(cs[c]); if (lane < 16) *(float*)(lds + CT_OFF + ((wm * 20 + wn * 5 + c) * 16 + li) * 4) = v; }
  }
  __syncthreads();
  f32x2 stt[4];
#pragma unroll
  for (int r = 0; r < 4; ++r) stt[r] = *(const f32x2*)(lds + ST_OFF + (64 * wm + 16 * r + li) * 8);
  f32x4 cv[5];
#pragma unroll
  for (int c = 0; c < 5; ++c) cv[c] = *(const f32x4*)(lds + CT_OFF + ((wn * 5 + c) * 16 + 4 * g) * 4) + *(const f32x4*)(lds + CT_OFF + ((20 + wn * 5 + c) * 16 + 4 * g) * 4);
  auto fold = [&](f32x4 a, f32x2 st, f32x4 c) { f32x4 r; for (int e = 0; e < 4; ++e) r[e] = (a[e] - st[0] * c[e]) * st[1]; return r; };
  const int rtg = 8 * mt + 4 * wm;
  char* a2b = (char*)a2;
  auto reglu = [&](f32x4 gt, f32x4 up) {
    u32x2 gp = pk4(gt), upk = pk4(up); f32x4 hv;
    hv[0] = fmaxf(lo2f(gp[0]), 0.f) * lo2f(upk[0]); hv[1] = fmaxf(hi2f(gp[0]), 0.f) * hi2f(upk[0]);
    hv[2] = fmaxf(lo2f(gp[1]), 0.f) * lo2f(upk[1]); hv[3] = fmaxf(hi2f(gp[1]), 0.f) * hi2f(upk[1]);
    return pk4(hv);
  };
  if constexpr (ISQ) {
    { int T16 = 4 * j + wn;
#pragma unroll
      for (int r = 0; r < 4; ++r) {
        u32x2 hv = reglu(fold(acc[r][3], stt[r], cv[3]), fold(acc[r][4], stt[r], cv[4]));
        *(u32x2*)(a2b + ((size_t)((rtg + r) * NKS + 16) * 2 + T16) * 512 + lane * 8) = hv;
      } }
    f32x2 hst;
    f32x2 hst4[4];
    if (wm) {
      hst = *(const f32x2*)(lds + ST_OFF + (40 + (li & 7) + 16 * (li >> 3)) * 8);
#pragma unroll
      for (int e = 0; e < 4; ++e) { int i = 4 * g + e; hst4[e] = *(const f32x2*)(lds + ST_OFF + (40 + (i & 7) + 16 * (i >> 3)) * 8); }
    } else {
      float a = 0, b = 0;
#pragma unroll
      for (int ww = 0; ww < 8; ++ww) { f32x2 v = *(const f32x2*)(lds + HP_OFF + (ww * 16 + li) * 8); a += v[0]; b += v[1]; }
      float mb, inv; finish_stats(a, b, mb, inv); hst = f32x2{mb, inv};
#pragma unroll
      for (int e = 0; e < 4; ++e) { hst4[e][0] = __shfl(mb, 4 * g + e); hst4[e][1] = __shfl(inv, 4 * g + e); }
    }
    float cvv = *(const float*)(lds + CT_OFF + ((wn * 5 + 2) * 16 + li) * 4) + *(const float*)(lds + CT_OFF + ((20 + wn * 5 + 2) * 16 + li) * 4);
    u32x2 Qp[4], Kp[4], Vp[4], Kh, Vh;
#pragma unroll
    for (int r = 0; r < 4; ++r) {
      Qp[r] = pk4(fold(acc[r][0], stt[r], cv[0]));
      Kp[r] = pk4(fold(acc[r][1], stt[r], cv[1]));
      f32x4 v;
#pragma unroll
      for (int e = 0; e < 4; ++e) { f32x2 st = *(const f32x2*)(lds + ST_OFF + (64 * wm + 16 * r + 4 * g + e) * 8); v[e] = (acc[r][2][e] - st[0] * cvv) * st[1]; }
      Vp[r] = pk4(v);
    }
    Kh = pk4(fold(acch[0], hst, cv[1]));
    { f32x4 v; for (int e = 0; e < 4; ++e) v[e] = (acch[1][e] - hst4[e][0] * cvv) * hst4[e][1]; Vh = pk4(v); }
    const float NEG = -1e30f;
    const bool nohalo = (mt == 0) && (wm == 0);
    f32x4 bcur, bprev, bh[2];
#pragma unroll
    for (int e = 0; e < 4; ++e) {
      int row = 4 * g + e;
      bcur[e] = (row <= li && row >= li - 7) ? 0.f : NEG;
      bprev[e] = (row >= li + 9) ? 0.f : NEG;
      bool hv = ((row & 7) >= li + 1) && !nohalo;
      bh[0][e] = (hv && row < 8) ? 0.f : NEG;
      bh[1][e] = (hv && row >= 8) ? 0.f : NEG;
    }
    const int head = 4 * j + wn;
    const float SC = 0.25f * 1.44269504088896f;
#pragma unroll
    for (int r = 0; r < 4; ++r) {
      f32x4 sc = mfma16(Kp[r], Qp[r], bcur);
      f32x4 sp = (r >= 2) ? mfma16(Kp[r - 2], Qp[r], bprev) : mfma16(Kh, Qp[r], bh[r & 1]);
      u32x2 scp = pk4(sc), spp = pk4(sp);
      float l[8] = {lo2f(spp[0]), hi2f(spp[0]), lo2f(spp[1]), hi2f(spp[1]), lo2f(scp[0]), hi2f(scp[0]), lo2f(scp[1]), hi2f(scp[1])};
      float mx = fmaxf(fmaxf(fmaxf(l[0], l[1]), fmaxf(l[2], l[3])), fmaxf(fmaxf(l[4], l[5]), fmaxf(l[6], l[7])));
      mx = gmax(mx);
      float sum = 0;
#pragma unroll
      for (int e = 0; e < 8; ++e) { l[e] = __builtin_amdgcn_exp2f((l[e] - mx) * SC); sum += l[e]; }
      sum = gsum(sum);
      float rs = 1.f / sum;
      u32x4 pb; pb[0] = pk2(l[0] * rs, l[1] * rs); pb[1] = pk2(l[2] * rs, l[3] * rs); pb[2] = pk2(l[4] * rs, l[5] * rs); pb[3] = pk2(l[6] * rs, l[7] * rs);
      u32x2 vprev = (r >= 2) ? Vp[r - 2] : Vh;
      u32x4 va; va[0] = vprev[0]; va[1] = vprev[1]; va[2] = Vp[r][0]; va[3] = Vp[r][1];
      f32x4 o = mfma32(va, pb, f32x4{0, 0, 0, 0});
      *(u32x2*)(a2b + ((size_t)((rtg + r) * NKS) * 2 + head) * 512 + lane * 8) = pk4(o);
    }
  } else {
    int T16 = (fa >> 4) + 2 * wn;
#pragma unroll
    for (int r = 0; r < 4; ++r) {
      u32x2 h0 = reglu(fold(acc[r][0], stt[r], cv[0]), fold(acc[r][2], stt[r], cv[2]));
      u32x2 h1 = reglu(fold(acc[r][1], stt[r], cv[1]), fold(acc[r][3], stt[r], cv[3]));
      char* p = a2b + ((size_t)((rtg + r) * NKS + 16) * 2 + T16) * 512 + lane * 8;
      *(u32x2*)p = h0; *(u32x2*)(p + 512) = h1;
    }
    int T16x = (fx + 8 * wn) >> 4;
#pragma unroll
    for (int rp = 0; rp < 2; ++rp) {
      f32x4 a = fold(acc[2 * rp][4], stt[2 * rp], cv[4]), b = fold(acc[2 * rp + 1][4], stt[2 * rp + 1], cv[4]);
      f32x4 gt, up;
#pragma unroll
      for (int e = 0; e < 4; ++e) {
        auto r = __builtin_amdgcn_permlane32_swap(__float_as_uint(a[e]), __float_as_uint(b[e]), false, false);
        gt[e] = __uint_as_float(r[0]); up[e] = __uint_as_float(r[1]);
      }
      u32x2 hv = reglu(gt, up);
      int rt = rtg + 2 * rp + (g >> 1), gp = 2 * (wn & 1) + (g & 1);
      *(u32x2*)(a2b + ((size_t)(rt * NKS + 16) * 2 + T16x) * 512 + (gp * 16 + li) * 8) = hv;
    }
  }
}

__global__ __launch_bounds__(512, 2) void k1(const u16* __restrict__ x, const u16* __restrict__ wqkv, const u16* __restrict__ wg,
                                             const u16* __restrict__ wu, u16* __restrict__ a2) {
  __shared__ __attribute__((aligned(16))) char lds[LDS1];
  const int bid = blockIdx.x, xcd = bid & 7, idx = bid >> 3;
  const int mt = (xcd & 1) * 8 + (idx >> 2), sl = idx & 3, nq = xcd >> 1;
  if (sl < 2) phase1<true>(x, wqkv, wg, wu, a2, lds, mt, 2 * nq + sl);
  else phase1<false>(x, wqkv, wg, wu, a2, lds, mt, 8 + 2 * nq + (sl - 2));
}

constexpr int LDS2 = 9 * 16384;
__global__ __launch_bounds__(512, 2) void k2(const u16* __restrict__ x, const u16* __restrict__ wo, const u16* __restrict__ wd,
                                             const u16* __restrict__ a2, u16* __restrict__ out) {
  __shared__ __attribute__((aligned(16))) char lds[LDS2];
  const int tid = threadIdx.x, lane = tid & 63, w = __builtin_amdgcn_readfirstlane(tid >> 6);
  const int li = lane & 15, g = lane >> 4;
  const int bid = blockIdx.x, xcd = bid & 7, idx = bid >> 3;
  const int mt2 = xcd * 4 + (idx >> 3), nt = idx & 7;
  const int ks0 = 9 * w;
  const char* ab = (const char*)a2 + (size_t)(4 * mt2 * NKS + ks0) * 1024 + lane * 8;
  const unsigned wlo = (unsigned)(4 * g * 1024 + nt * 128 + li * 8);
  constexpr int D = 4;
  u32x2 ar[D][4][2], br[D][8];
  f32x4 acc[4][4];
#pragma unroll
  for (int r = 0; r < 4; ++r)
#pragma unroll
    for (int c = 0; c < 4; ++c) acc[r][c] = f32x4{0, 0, 0, 0};
  auto load = [&](auto sc) {
    constexpr int s = sc; constexpr int b = s % D;
    int ks = ks0 + s;
    const char* wb = (ks < 16) ? (const char*)wo + (size_t)ks * 32768 : (const char*)wd + (size_t)(ks - 16) * 32768;
#pragma unroll
    for (int jj = 0; jj < 8; ++jj) br[b][jj] = *(const u32x2*)(wb + wlo + (16 * (jj >> 2) + (jj & 3)) * 1024);
#pragma unroll
    for (int r = 0; r < 4; ++r) {
      ar[b][r][0] = *(const u32x2*)(ab + (size_t)(r * NKS + s) * 1024);
      ar[b][r][1] = *(const u32x2*)(ab + (size_t)(r * NKS + s) * 1024 + 512);
    }
  };
  auto flush = [&](int slot) {
#pragma unroll
    for (int r = 0; r < 4; ++r)
#pragma unroll
      for (int e = 0; e < 4; ++e) {
        f32x4 v = {acc[r][0][e], acc[r][1][e], acc[r][2][e], acc[r][3][e]};
        *(f32x4*)(lds + slot * 16384 + (16 * r + 4 * g + e) * 256 + li * 16) = v;
      }
  };
  static_for<D>([&](auto sc) { load(sc); });
  const int erow = tid >> 3, ecol = (tid & 7) * 8;
  const int rho = 64 * mt2 + erow;
  const int tok = 32 * (rho >> 5) + 2 * (rho & 15) + ((rho >> 4) & 1);
  const u32x4 xv = *(const u32x4*)(x + (size_t)tok * 512 + nt * 64 + ecol);
  static_for<9>([&](auto sc) {
    constexpr int s = sc; constexpr int b = s % D;
    if constexpr (s == 7) {
      if (w == 1) {
        flush(8);
#pragma unroll
        for (int r = 0; r < 4; ++r)
#pragma unroll
          for (int c = 0; c < 4; ++c) acc[r][c] = f32x4{0, 0, 0, 0};
      }
    }
    u32x4 bfr[4];
#pragma unroll
    for (int c = 0; c < 4; ++c)
#pragma unroll
      for (int m = 0; m < 4; ++m)
        bfr[c][m] = __builtin_amdgcn_perm(br[b][2 * m + 1][c >> 1], br[b][2 * m][c >> 1], (c & 1) ? 0x07060302u : 0x05040100u);
    u32x4 afr[4];
#pragma unroll
    for (int r = 0; r < 4; ++r) { afr[r][0] = ar[b][r][0][0]; afr[r][1] = ar[b][r][0][1]; afr[r][2] = ar[b][r][1][0]; afr[r][3] = ar[b][r][1][1]; }
#pragma unroll
    for (int r = 0; r < 4; ++r)
#pragma unroll
      for (int c = 0; c < 4; ++c) acc[r][c] = mfma32(afr[r], bfr[c], acc[r][c]);
    if constexpr (s + D < 9) load(std::integral_constant<int, s + D>{});
  });
  flush(w);
  __syncthreads();
  const char* pb = lds + erow * 256 + ecol * 4;
  f32x4 sa = *(const f32x4*)(pb) + *(const f32x4*)(pb + 8 * 16384);
  f32x4 sb = *(const f32x4*)(pb + 16) + *(const f32x4*)(pb + 8 * 16384 + 16);
  f32x4 ga = {0, 0, 0, 0}, gb = {0, 0, 0, 0};
#pragma unroll
  for (int k = 1; k < 8; ++k) { ga += *(const f32x4*)(pb + k * 16384); gb += *(const f32x4*)(pb + k * 16384 + 16); }
  float sv[8] = {sa[0], sa[1], sa[2], sa[3], sb[0], sb[1], sb[2], sb[3]};
  float gv[8] = {ga[0], ga[1], ga[2], ga[3], gb[0], gb[1], gb[2], gb[3]};
  float ov[8];
#pragma unroll
  for (int e = 0; e < 8; ++e) {
    float d = rbf(gv[e]);
    float si = rbf(d / (1.f + __expf(-d)));
    float xx = (e & 1) ? hi2f(xv[e >> 1]) : lo2f(xv[e >> 1]);
    ov[e] = xx + rbf(rbf(sv[e]) * si);
  }
  u32x4 o; o[0] = pk2(ov[0], ov[1]); o[1] = pk2(ov[2], ov[3]); o[2] = pk2(ov[4], ov[5]); o[3] = pk2(ov[6], ov[7]);
  *(u32x4*)(out + (size_t)tok * 512 + nt * 64 + ecol) = o;
}

extern "C" void kernel_launch(void* const* d_in, const int* in_sizes, int n_in, void* d_out, int out_size, void* d_ws, size_t ws_size,
                              hipStream_t stream) {
  const u16* x = (const u16*)d_in[0]; const u16* wqkv = (const u16*)d_in[1]; const u16* wo = (const u16*)d_in[2];
  const u16* wg = (const u16*)d_in[4]; const u16* wu = (const u16*)d_in[5]; const u16* wd = (const u16*)d_in[6];
  u16* a2 = (u16*)d_ws;
  k1<<<256, 512, 0, stream>>>(x, wqkv, wg, wu, a2);
  k2<<<256, 512, 0, stream>>>(x, wo, wd, a2, (u16*)d_out);
}
```

```cpp
#include <hip/hip_runtime.h>
#include <utility>
#define FUSED 1
typedef unsigned short u16;
typedef unsigned long long u64;
typedef __attribute__((ext_vector_type(4))) short s16x4;
typedef __attribute__((ext_vector_type(4))) float f32x4;
typedef __attribute__((ext_vector_type(2))) float f32x2;
typedef __attribute__((ext_vector_type(2))) __bf16 bf16x2;
typedef __attribute__((ext_vector_type(8))) __bf16 bf16x8;
typedef __attribute__((ext_vector_type(2))) unsigned u32x2;
typedef __attribute__((ext_vector_type(4))) unsigned u32x4;
#define LAS __attribute__((address_space(3)))
#define GAS __attribute__((address_space(1)))
#define DI __device__ __forceinline__
#define SCHED __builtin_amdgcn_sched_barrier(0)

template <int... I, class F> DI void static_for_impl(std::integer_sequence<int, I...>, F&& f) { (f(std::integral_constant<int, I>{}), ...); }
template <int N, class F> DI void static_for(F&& f) { static_for_impl(std::make_integer_sequence<int, N>{}, f); }
template <int N> using IC = std::integral_constant<int, N>;

template <int N> DI void wait_vm() { asm volatile("s_waitcnt vmcnt(%0)" ::"i"(N) : "memory"); }
DI void wait_lgkm0() { asm volatile("s_waitcnt lgkmcnt(0)" ::: "memory"); }
DI void bar() { SCHED; __builtin_amdgcn_s_barrier(); SCHED; }
DI void dma16(unsigned lds_dst, unsigned voff, const void* sbase) {
  asm volatile("s_mov_b32 m0, %0\n\ts_nop 0\n\tglobal_load_lds_dwordx4 %1, %2" :: "s"(lds_dst), "v"(voff), "s"(sbase) : "memory");
}
DI void gload_sc1(u32x4& d, unsigned voff, const void* sbase) {
  asm volatile("global_load_dwordx4 %0, %1, %2 sc1" : "=v"(d) : "v"(voff), "s"(sbase) : "memory");
}
DI void gstore_sc1(u32x4 d, unsigned voff, void* sbase) {
  asm volatile("global_store_dwordx4 %0, %1, %2 sc1\n\ts_nop 1" :: "v"(voff), "v"(d), "s"(sbase) : "memory");
}
DI f32x4 mfma32(u32x4 a, u32x4 b, f32x4 c) {
  return __builtin_amdgcn_mfma_f32_16x16x32_bf16(__builtin_bit_cast(bf16x8, a), __builtin_bit_cast(bf16x8, b), c, 0, 0, 0);
}
DI f32x4 mfma16(u32x2 a, u32x2 b, f32x4 c) {
  return __builtin_amdgcn_mfma_f32_16x16x16bf16_1k(__builtin_bit_cast(s16x4, a), __builtin_bit_cast(s16x4, b), c, 0, 0, 0);
}
DI f32x4 mfmaf(float a, float b) { return __builtin_amdgcn_mfma_f32_16x16x4f32(a, b, f32x4{0, 0, 0, 0}, 0, 0, 0); }
DI float dot2(unsigned a, unsigned b, float c) {
  return __builtin_amdgcn_fdot2_f32_bf16(__builtin_bit_cast(bf16x2, a), __builtin_bit_cast(bf16x2, b), c, false);
}
DI unsigned pk2(float lo, float hi) { bf16x2 c; c[0] = (__bf16)lo; c[1] = (__bf16)hi; return __builtin_bit_cast(unsigned, c); }
DI float lo2f(unsigned u) { return __uint_as_float(u << 16); }
DI float hi2f(unsigned u) { return __uint_as_float(u & 0xffff0000u); }
DI float rbf(float f) { return lo2f(pk2(f, f)); }
DI u32x2 pk4(f32x4 v) { u32x2 r; r[0] = pk2(v[0], v[1]); r[1] = pk2(v[2], v[3]); return r; }
DI u32x4 cat(u32x2 a, u32x2 b) { u32x4 r; r[0] = a[0]; r[1] = a[1]; r[2] = b[0]; r[3] = b[1]; return r; }
DI float gsum(float v) {
  unsigned u = __float_as_uint(v);
  auto r = __builtin_amdgcn_permlane32_swap(u, u, false, false);
  v = __uint_as_float(r[0]) + __uint_as_float(r[1]);
  u = __float_as_uint(v);
  auto r2 = __builtin_amdgcn_permlane16_swap(u, u, false, false);
  return __uint_as_float(r2[0]) + __uint_as_float(r2[1]);
}
DI float gmax(float v) {
  unsigned u = __float_as_uint(v);
  auto r = __builtin_amdgcn_permlane32_swap(u, u, false, false);
  v = fmaxf(__uint_as_float(r[0]), __uint_as_float(r[1]));
  u = __float_as_uint(v);
  auto r2 = __builtin_amdgcn_permlane16_swap(u, u, false, false);
  return fmaxf(__uint_as_float(r2[0]), __uint_as_float(r2[1]));
}
DI u32x4 lds_b128(const char* lds, unsigned off) { return *(const u32x4*)(lds + off); }
DI u32x4 lds_tr2(const char* lds, unsigned off, unsigned step) {
  s16x4 t0 = __builtin_amdgcn_ds_read_tr16_b64_v4i16((LAS s16x4*)(lds + off));
  s16x4 t1 = __builtin_amdgcn_ds_read_tr16_b64_v4i16((LAS s16x4*)(lds + off + step));
  return cat(__builtin_bit_cast(u32x2, t0), __builtin_bit_cast(u32x2, t1));
}

constexpr int A_STAGE = 18432, B_STAGE = 20480;
constexpr int B_OFF = 0, A_OFF = 5 * B_STAGE;
constexpr int T_OFF = A_OFF + 3 * A_STAGE;
constexpr int SM_OFF = T_OFF;
constexpr int SI_OFF = SM_OFF + 512;
constexpr int HP_OFF = SI_OFF + 512;
constexpr int LDS_BYTES = HP_OFF + 1024;
constexpr int NK16 = 144;
constexpr size_t SYNC_OFF = 16u << 20;
struct Params { const u16 *x, *wqkv, *wo, *wg, *wu, *wd; u16* out; char* ws; };

#define STAMP(i)

DI void finish_stats(float s1, float s2, float& mb, float& inv) {
  float mean = s1 * (1.f / 512.f);
  float var = fmaxf(s2 * (1.f / 512.f) - mean * mean, 0.f);
  mb = rbf(mean);
  float sd = rbf(__builtin_amdgcn_sqrtf(rbf(rbf(var) + 1e-5f)));
  inv = __builtin_amdgcn_rcpf(sd);
}
DI f32x4 foldm(f32x4 a, float m, f32x4 c) { return a - m * c; }

DI void issueW2(const Params& p, unsigned lbase, int w, int slot, int ks, unsigned wvo, int nt) {
  const char* wb = ((ks < 16) ? (const char*)p.wo + (size_t)ks * 32768 : (const char*)p.wd + (size_t)(ks - 16) * 32768) + nt * 128;
  unsigned dst = lbase + w * 16384 + slot * 4096;
#pragma unroll
  for (int i = 0; i < 4; ++i) dma16(dst + i * 1024, wvo, wb + i * 8192);
}

template <bool ISQ, bool PF2>
DI void phase1(const Params& p, char* lds, int mt, int j, int ks0, unsigned wvo, int nt) {
  const int tid = threadIdx.x, lane = tid & 63;
  const int w = __builtin_amdgcn_readfirstlane(tid >> 6), wm = w >> 2, wn = w & 3;
  const int li = lane & 15, g = lane >> 4;
  const unsigned lbase = (unsigned)(size_t)lds;
  int fa = 0, fx = 0;
  if (!ISQ) { int i = j - 8, f0 = 512 + 160 * i; if (i & 1) { fx = f0; fa = f0 + 32; } else { fa = f0; fx = f0 + 128; } }
  const char* sb0; const char* sb1;
  unsigned vo0, vo1;
  unsigned ld = 0;
  const bool bw = w < 5;
  const bool mixed = (!ISQ) && (w == 4);
  const int T0 = mt * 128;
  if (bw) {
    if (ISQ) { ld = w < 3 ? 3072 : 3584; sb0 = (w < 3) ? (const char*)(p.wqkv + w * 512 + 64 * j) : (const char*)((w == 3 ? p.wg : p.wu) + 64 * j); }
    else { ld = 3584; sb0 = (w < 4) ? (const char*)((w < 2 ? p.wg : p.wu) + fa + 64 * (w & 1)) : (const char*)(p.wg + fx); }
    sb1 = mixed ? (const char*)(p.wu + fx) : sb0;
    if (!mixed) {
      int q = lane >> 3, slot = lane & 7, jp = slot >> 1, e = slot & 1;
      int s0 = (q >> 1) & 1, s1 = s0 | 2;
      vo0 = q * ld + 16 * (2 * (jp ^ s0) + e);
      vo1 = (q + 8) * ld + 16 * (2 * (jp ^ s1) + e);
    } else {
      int r16 = lane >> 2, cp = lane & 3, s0 = (r16 >> 3) & 1;
      vo0 = r16 * ld + 16 * (cp ^ s0);
      vo1 = r16 * ld + 16 * (cp ^ (s0 | 2));
    }
  } else {
    int lrow = lane >> 3, slot = lane & 7;
    vo0 = lrow * 2048 + 16 * (slot ^ (lrow >> 1));
    vo1 = lrow * 2048 + 16 * (slot ^ (4 + (lrow >> 1)));
    sb0 = (const char*)p.x; sb1 = sb0;
  }
  unsigned atb[6];
#pragma unroll
  for (int i = 0; i < 6; ++i) {
    int pc = 6 * (w - 5) + i;
    int tb = pc < 16 ? T0 + 32 * (pc >> 2) + 16 * (pc & 1) + ((pc >> 1) & 1) : (mt ? T0 - 16 : 0) + (pc & 1);
    atb[i] = bw ? 0u : (unsigned)tb * 1024u;
  }
  auto issueB = [&](int t) {
    unsigned dst = lbase + B_OFF + (t % 5) * B_STAGE + w * 4096;
    size_t ko = (size_t)(32 * t) * ld, h16 = (size_t)16 * ld;
    dma16(dst, vo0, sb0 + ko);
    dma16(dst + 1024, vo1, sb1 + ko);
    dma16(dst + 2048, vo0, sb0 + ko + h16);
    dma16(dst + 3072, vo1, sb1 + ko + h16);
  };
  auto issueA = [&](int s, auto hc) {
    constexpr int half = hc;
#pragma unroll
    for (int i = 0; i < 3; ++i) {
      constexpr int dummy = 0; (void)dummy;
      int ii = 3 * half + i;
      unsigned dst = lbase + A_OFF + (s % 3) * A_STAGE + (6 * (w - 5) + ii) * 1024;
      dma16(dst, (ii & 1) ? vo1 : vo0, sb0 + atb[ii] + s * 128);
    }
  };
  if (bw) { issueB(0); issueB(1); issueB(2); }
  else { issueA(0, IC<0>{}); issueA(0, IC<1>{}); issueA(1, IC<0>{}); issueA(1, IC<1>{}); }

  unsigned aoff[2], hoff[2], h2off[2], soff[2], boff[2][3];
  { int rho = 64 * wm + li; int f = (rho >> 1) & 7; aoff[0] = A_OFF + rho * 128 + 16 * (g ^ f); aoff[1] = A_OFF + rho * 128 + 16 * ((4 + g) ^ f); }
  { int rho = wm ? (40 + (li & 7) + 16 * (li >> 3)) : 128 + li; int f = (rho >> 1) & 7; hoff[0] = A_OFF + rho * 128 + 16 * (g ^ f); hoff[1] = A_OFF + rho * 128 + 16 * ((4 + g) ^ f); }
  { int rho = 128 + li; int f = (rho >> 1) & 7; h2off[0] = A_OFF + rho * 128 + 16 * (g ^ f); h2off[1] = A_OFF + rho * 128 + 16 * ((4 + g) ^ f); }
  soff[0] = aoff[0] + wn * 2048; soff[1] = aoff[1] + wn * 2048;
  { int q = (lane >> 2) & 3, pp = lane & 3, s = ((q >> 1) & 1) | ((g & 1) << 1);
    unsigned rb = B_OFF + g * 1024 + 128 * q + 8 * pp;
    if (ISQ) {
      boff[0][0] = rb + 32 * (wn ^ s); boff[0][1] = boff[0][2] = 0;
    } else {
      int p0 = wn >> 1, j0 = 2 * (wn & 1);
      boff[0][0] = rb + p0 * 4096 + 32 * (j0 ^ s); boff[0][1] = rb + p0 * 4096 + 32 * ((j0 + 1) ^ s);
      int up = pp >> 1, d = 2 * (g >> 1) + up, r16 = 8 * (g & 1) + q, sg = (g & 1) | (up << 1);
      boff[0][2] = B_OFF + 16384 + d * 1024 + 64 * r16 + 16 * (wn ^ sg) + 8 * (pp & 1);
    }
#pragma unroll
    for (int c = 0; c < (ISQ ? 1 : 3); ++c) { boff[1][c] = boff[0][c] + 3 * B_STAGE; asm volatile("" : "+v"(boff[0][c]), "+v"(boff[1][c])); }
  }
  asm volatile("" : "+v"(aoff[0]), "+v"(aoff[1]), "+v"(soff[0]), "+v"(soff[1]));
  if (ISQ) asm volatile("" : "+v"(hoff[0]), "+v"(hoff[1]), "+v"(h2off[0]), "+v"(h2off[1]));
  f32x4 acc[4][5]; f32x4 acch[2];
#pragma unroll
  for (int r = 0; r < 4; ++r)
#pragma unroll
    for (int c = 0; c < 5; ++c) acc[r][c] = f32x4{0, 0, 0, 0};
  acch[0] = f32x4{0, 0, 0, 0}; acch[1] = f32x4{0, 0, 0, 0};
  float s1 = 0, s2 = 0, hs1 = 0, hs2 = 0, cs[5] = {0, 0, 0, 0, 0};
  const unsigned ONES = 0x3f803f80u;
  u32x4 af[4], ah, as_, bf[5];

  if (bw) wait_vm<8>(); else wait_vm<6>();
  bar();
  STAMP(1);
  if (wm) bar();
  static_for<16>([&](auto tc) {
    constexpr int t = tc; constexpr int P = t & 1;
    constexpr int L = (t + 4 < 15) ? t + 4 : 15;
    constexpr int nB = (t < 15) ? (L - (t + 1)) * 4 : 0;
    constexpr int issA = 18 + 3 * ((t - 1 < 0) ? 0 : (t - 1 > 10 ? 10 : t - 1));
    constexpr int nA = (t < 15) ? issA - 6 * (((t + 1) >> 1) + 1) : 0;
    {
      constexpr int ao = ((t >> 1) % 3) * A_STAGE, bh_ = (t % 5) >= 3, bo = ((t % 5) - 3 * bh_) * B_STAGE;
#pragma unroll
      for (int c = 0; c < 5; ++c) {
        if (ISQ) bf[c] = lds_tr2(lds, boff[bh_][0] + bo + c * 4096, 512);
        else if (c < 4) bf[c] = lds_tr2(lds, boff[bh_][c & 1] + bo + (c >> 1) * 8192, 512);
        else bf[c] = lds_tr2(lds, boff[bh_][2] + bo, 256);
      }
#pragma unroll
      for (int r = 0; r < 4; ++r) af[r] = lds_b128(lds, aoff[P] + ao + r * 2048);
      if (ISQ) ah = lds_b128(lds, hoff[P] + ao);
      as_ = lds_b128(lds, soff[P] + ao);
      SCHED;
      if (bw) {
        if constexpr (t == 0) { issueB(3); issueB(4); }
        else if constexpr (t + 4 < 16) issueB(t + 4);
      } else {
        if constexpr (t == 0) { issueA(2, IC<0>{}); issueA(2, IC<1>{}); }
        else if constexpr (t >= 2 && t <= 11) issueA((t - 2) / 2 + 3, IC<t & 1>{});
      }
      if (ISQ && w == (t >> 1)) {
        u32x4 hv = lds_b128(lds, h2off[P] + ao);
#pragma unroll
        for (int e = 0; e < 4; ++e) { hs1 = dot2(hv[e], ONES, hs1); hs2 = dot2(hv[e], hv[e], hs2); }
      }
      if (wm) { if (bw) wait_vm<nB>(); else wait_vm<nA>(); }
      wait_lgkm0();
    }
    bar();
    __builtin_amdgcn_s_setprio(1);
#pragma unroll
    for (int r = 0; r < 4; ++r)
#pragma unroll
      for (int c = 0; c < 5; ++c) {
        if (ISQ && c == 2) acc[r][c] = mfma32(af[r], bf[c], acc[r][c]);
        else acc[r][c] = mfma32(bf[c], af[r], acc[r][c]);
        cs[c] = dot2(bf[c][r], ONES, cs[c]);
        if (c < 2) { if (c == 0) s1 = dot2(as_[r], ONES, s1); else s2 = dot2(as_[r], as_[r], s2); }
      }
    if (ISQ) { acch[0] = mfma32(bf[1], ah, acch[0]); acch[1] = mfma32(ah, bf[2], acch[1]); }
    __builtin_amdgcn_s_setprio(0);
    asm volatile("" : "+v"(s1), "+v"(s2), "+v"(cs[0]), "+v"(cs[1]), "+v"(cs[2]), "+v"(cs[3]), "+v"(cs[4]));
    if (!wm) wait_vm<nB>();
    if constexpr (t < 15) bar();
  });
  if (!wm) bar();
  STAMP(18);
  {
    f32x4 t1 = mfmaf(1.f, s1), t2 = mfmaf(1.f, s2);
    float mb, inv;
    finish_stats(t1[0], t2[0], mb, inv);
    *(float*)(lds + SM_OFF + (16 * w + li) * 4) = mb;
    *(float*)(lds + SI_OFF + (16 * w + li) * 4) = inv;
    if (ISQ) { f32x4 h1 = mfmaf(1.f, hs1), h2 = mfmaf(1.f, hs2); *(f32x2*)(lds + HP_OFF + (w * 16 + li) * 8) = f32x2{h1[0], h2[0]}; }
  }
  f32x4 cv[5];
#pragma unroll
  for (int c = 0; c < 5; ++c) cv[c] = (ISQ && c == 2) ? mfmaf(1.f, cs[c]) : mfmaf(cs[c], 1.f);
  wait_lgkm0();
  bar();
  STAMP(19);
  if (PF2) {
#pragma unroll
    for (int s = 0; s < 4; ++s) issueW2(p, lbase, w, s, ks0 + s, wvo, nt);
  }
  float sm[4], si[4];
#pragma unroll
  for (int r = 0; r < 4; ++r) { sm[r] = *(const float*)(lds + SM_OFF + (64 * wm + 16 * r + li) * 4); si[r] = *(const float*)(lds + SI_OFF + (64 * wm + 16 * r + li) * 4); }
  char* a2b = p.ws + (size_t)(4 * mt + 2 * wm) * NK16 * 1024;
  const unsigned lo16 = lane * 16;
  auto reglu = [&](f32x4 ga, f32x4 ua, float inv) {
    f32x4 t; for (int e = 0; e < 4; ++e) t[e] = fmaxf(ga[e], 0.f);
    return pk4(t * (ua * (inv * inv)));
  };
  if constexpr (ISQ) {
    {
      u32x2 hv[4];
#pragma unroll
      for (int r = 0; r < 4; ++r) hv[r] = reglu(foldm(acc[r][3], sm[r], cv[3]), foldm(acc[r][4], sm[r], cv[4]), si[r]);
      char* pb = a2b + (size_t)(32 + 4 * j + wn) * 1024;
      gstore_sc1(cat(hv[0], hv[1]), lo16, pb);
      gstore_sc1(cat(hv[2], hv[3]), lo16, pb + NK16 * 1024);
    }
    float hm, hi; f32x4 hm4, hi4;
    if (wm) {
      int rr = 40 + (li & 7) + 16 * (li >> 3), r4 = 40 + 4 * (g & 1) + 16 * (g >> 1);
      hm = *(const float*)(lds + SM_OFF + rr * 4); hi = *(const float*)(lds + SI_OFF + rr * 4);
      hm4 = *(const f32x4*)(lds + SM_OFF + r4 * 4); hi4 = *(const f32x4*)(lds + SI_OFF + r4 * 4);
    } else {
      f32x2 a = {0, 0};
#pragma unroll
      for (int ww = 0; ww < 8; ++ww) a += *(const f32x2*)(lds + HP_OFF + (ww * 16 + li) * 8);
      finish_stats(a[0], a[1], hm, hi);
      float z = g == 0 ? 1.f : 0.f;
      hm4 = mfmaf(hm, z); hi4 = mfmaf(hi, z);
    }
    u32x2 Qp[4], Kp[4], Vp[4], Kh, Vh;
#pragma unroll
    for (int r = 0; r < 4; ++r) {
      Qp[r] = pk4(foldm(acc[r][0], sm[r], cv[0]) * si[r]);
      Kp[r] = pk4(foldm(acc[r][1], sm[r], cv[1]) * si[r]);
      f32x4 m4 = *(const f32x4*)(lds + SM_OFF + (64 * wm + 16 * r + 4 * g) * 4), i4 = *(const f32x4*)(lds + SI_OFF + (64 * wm + 16 * r + 4 * g) * 4);
      Vp[r] = pk4((acc[r][2] - m4 * cv[2]) * i4);
    }
    Kh = pk4(foldm(acch[0], hm, cv[1]) * hi);
    Vh = pk4((acch[1] - hm4 * cv[2]) * hi4);
    const float NEG = -1e30f;
    const bool nohalo = (mt == 0) && (wm == 0);
    f32x4 bcur, bprev, bh[2];
#pragma unroll
    for (int e = 0; e < 4; ++e) {
      int row = 4 * g + e;
      bcur[e] = (row <= li && row >= li - 7) ? 0.f : NEG;
      bprev[e] = (row >= li + 9) ? 0.f : NEG;
      bool hv = ((row & 7) >= li + 1) && !nohalo;
      bh[0][e] = (hv && row < 8) ? 0.f : NEG;
      bh[1][e] = (hv && row >= 8) ? 0.f : NEG;
    }
    const float SC = 0.25f * 1.44269504088896f;
    u32x2 ov[4];
#pragma unroll
    for (int r = 0; r < 4; ++r) {
      f32x4 sc = mfma16(Kp[r], Qp[r], bcur);
      f32x4 sp = (r >= 2) ? mfma16(Kp[r - 2], Qp[r], bprev) : mfma16(Kh, Qp[r], bh[r & 1]);
      u32x2 scp = pk4(sc), spp = pk4(sp);
      float l[8] = {lo2f(spp[0]), hi2f(spp[0]), lo2f(spp[1]), hi2f(spp[1]), lo2f(scp[0]), hi2f(scp[0]), lo2f(scp[1]), hi2f(scp[1])};
      float mx = fmaxf(fmaxf(fmaxf(l[0], l[1]), fmaxf(l[2], l[3])), fmaxf(fmaxf(l[4], l[5]), fmaxf(l[6], l[7])));
      mx = gmax(mx) * SC;
      float sum = 0;
#pragma unroll
      for (int e = 0; e < 8; ++e) { l[e] = __builtin_amdgcn_exp2f(l[e] * SC - mx); sum += l[e]; }
      sum = gsum(sum);
      float rs = __builtin_amdgcn_rcpf(sum);
      u32x4 pb; pb[0] = pk2(l[0] * rs, l[1] * rs); pb[1] = pk2(l[2] * rs, l[3] * rs); pb[2] = pk2(l[4] * rs, l[5] * rs); pb[3] = pk2(l[6] * rs, l[7] * rs);
      f32x4 o = mfma32(cat((r >= 2) ? Vp[r - 2] : Vh, Vp[r]), pb, f32x4{0, 0, 0, 0});
      ov[r] = pk4(o);
    }
    char* pb = a2b + (size_t)(4 * j + wn) * 1024;
    gstore_sc1(cat(ov[0], ov[1]), lo16, pb);
    gstore_sc1(cat(ov[2], ov[3]), lo16, pb + NK16 * 1024);
  } else {
    char* pb = a2b + (size_t)(32 + (fa >> 4) + 2 * wn) * 1024;
#pragma unroll
    for (int c = 0; c < 2; ++c) {
      u32x2 hv[4];
#pragma unroll
      for (int r = 0; r < 4; ++r) hv[r] = reglu(foldm(acc[r][c], sm[r], cv[c]), foldm(acc[r][2 + c], sm[r], cv[2 + c]), si[r]);
      gstore_sc1(cat(hv[0], hv[1]), lo16, pb + c * 1024);
      gstore_sc1(cat(hv[2], hv[3]), lo16, pb + c * 1024 + NK16 * 1024);
    }
    u32x2 hx[2];
#pragma unroll
    for (int rp = 0; rp < 2; ++rp) {
      f32x4 a = foldm(acc[rp][4], sm[rp], cv[4]) * si[rp], b = foldm(acc[rp + 2][4], sm[rp + 2], cv[4]) * si[rp + 2];
      f32x4 gt, up;
#pragma unroll
      for (int e = 0; e < 4; ++e) {
        auto r = __builtin_amdgcn_permlane32_swap(__float_as_uint(a[e]), __float_as_uint(b[e]), false, false);
        gt[e] = __uint_as_float(r[0]); up[e] = __uint_as_float(r[1]);
      }
      hx[rp] = reglu(gt, up, 1.f);
    }
    unsigned vo = (unsigned)((g >> 1) * NK16 * 1024 + ((2 * (wn & 1) + (g & 1)) * 16 + li) * 16);
    gstore_sc1(cat(hx[0], hx[1]), vo, a2b + (size_t)(32 + ((fx + 8 * wn) >> 4)) * 1024);
  }
}

template <int MODE>
__global__ __launch_bounds__(512, 2) void fused(Params p) {
  __shared__ __attribute__((aligned(16))) char lds[LDS_BYTES];
  const int tid = threadIdx.x, lane = tid & 63, w = __builtin_amdgcn_readfirstlane(tid >> 6);
  const int li = lane & 15, g = lane >> 4;
  const int bid = blockIdx.x, xcd = bid & 7, idx = bid >> 3;
  const int mt = (xcd & 3) * 4 + (idx >> 3), s8 = idx & 7, nh = xcd >> 2;
  const int nt = s8;
  const unsigned lbase = (unsigned)(size_t)lds;
  STAMP(0);
  const int ks0 = w < 2 ? 8 * w : (w < 6 ? 16 + 9 * (w - 2) : 52 + 10 * (w - 6));
  const int nst = w < 2 ? 8 : (w < 6 ? 9 : 10);
  unsigned wvo;
  { int q = lane >> 3, slot = lane & 7; wvo = q * 1024 + 16 * (2 * ((slot >> 1) ^ ((q >> 1) & 3)) + (slot & 1)); }
  GAS u64* epoch_p = (GAS u64*)(p.ws + SYNC_OFF + mt * 512);
  GAS u64* flags = epoch_p + 32;
  u64 tag = 0;
  if (MODE == 0) tag = __hip_atomic_load(epoch_p, __ATOMIC_RELAXED, __HIP_MEMORY_SCOPE_AGENT) + 1;
  if (MODE != 2) {
    if (s8 < 4) phase1<true, MODE == 0>(p, lds, mt, 4 * nh + s8, ks0, wvo, nt);
    else phase1<false, MODE == 0>(p, lds, mt, 8 + 4 * nh + (s8 - 4), ks0, wvo, nt);
    STAMP(20);
    if (MODE == 1) return;
    wait_vm<0>();
    bar();
    if (tid == 0) __hip_atomic_store(flags + nh * 8 + s8, tag, __ATOMIC_RELAXED, __HIP_MEMORY_SCOPE_AGENT);
    STAMP(21);
  } else {
#pragma unroll
    for (int s = 0; s < 4; ++s) issueW2(p, lbase, w, s, ks0 + s, wvo, nt);
    wait_vm<0>();
  }
  unsigned troff[4];
  { int q = (lane >> 2) & 3, pp = lane & 3, qq = 4 * (g & 1) + q;
#pragma unroll
    for (int c = 0; c < 4; ++c) troff[c] = w * 16384 + (g >> 1) * 1024 + 128 * qq + 32 * (c ^ ((qq >> 1) & 3)) + 8 * pp; }
  u32x4 breg[4][4];
#pragma unroll
  for (int s = 0; s < 4; ++s)
#pragma unroll
    for (int c = 0; c < 4; ++c) breg[s][c] = lds_tr2(lds, troff[c] + s * 4096, 2048);
  wait_lgkm0();
#pragma unroll
  for (int s = 0; s < 4; ++s)
#pragma unroll
    for (int c = 0; c < 4; ++c) asm volatile("" : "+v"(breg[s][c]));
#pragma unroll
  for (int s = 0; s < 4; ++s) issueW2(p, lbase, w, s, ks0 + 4 + s, wvo, nt);
  const int erow = tid >> 3, ecol = (tid & 7) * 8;
  const int rho = 128 * mt + 64 * nh + erow;
  const int tok = 32 * (rho >> 5) + 2 * (rho & 15) + ((rho >> 4) & 1);
  u32x4 xv;
  asm volatile("global_load_dwordx4 %0, %1, %2" : "=v"(xv) : "v"((unsigned)(tok * 1024 + nt * 128 + ecol * 2)), "s"(p.x) : "memory");
  if (MODE == 0) {
    if (w == 0) {
      for (unsigned spins = 0; spins < (1u << 20); ++spins) {
        u64 v = __hip_atomic_load(flags + li, __ATOMIC_RELAXED, __HIP_MEMORY_SCOPE_AGENT);
        if (__all(v == tag)) break;
        __builtin_amdgcn_s_sleep(1);
      }
    }
    bar();
  }
  STAMP(22);
  const char* ab = p.ws + ((size_t)(4 * mt + 2 * nh) * NK16 + 2 * ks0) * 1024;
  const unsigned lo16 = lane * 16;
  constexpr int D = 5;
  u32x4 ar[D][4];
  f32x4 acc[4][4];
#pragma unroll
  for (int r = 0; r < 4; ++r)
#pragma unroll
    for (int c = 0; c < 4; ++c) acc[r][c] = f32x4{0, 0, 0, 0};
  auto loadA = [&](auto sc) {
    constexpr int s = sc; constexpr int b = s % D;
    gload_sc1(ar[b][0], lo16, ab + s * 2048);
    gload_sc1(ar[b][1], lo16, ab + s * 2048 + 1024);
    gload_sc1(ar[b][2], lo16, ab + s * 2048 + NK16 * 1024);
    gload_sc1(ar[b][3], lo16, ab + s * 2048 + NK16 * 1024 + 1024);
  };
  static_for<D>([&](auto sc) { loadA(sc); });
  static_for<10>([&](auto sc) {
    constexpr int s = sc; constexpr int b = s % D;
    if (s < 8 || s < nst) {
      constexpr int N = (s <= 2) ? 16 : (s == 3 ? 16 : (s == 4 ? 12 : (s == 5 ? 8 : (s == 6 ? 4 : 0))));
      wait_vm<N>();
      asm volatile("" : "+v"(ar[b][0]), "+v"(ar[b][1]), "+v"(ar[b][2]), "+v"(ar[b][3]));
      u32x4 bfr[4];
      if constexpr (s < 4) {
#pragma unroll
        for (int c = 0; c < 4; ++c) bfr[c] = breg[s][c];
      } else {
#pragma unroll
        for (int c = 0; c < 4; ++c) bfr[c] = lds_tr2(lds, troff[c] + (s & 3) * 4096, 2048);
      }
      u32x4 afr[4];
#pragma unroll
      for (int pr = 0; pr < 2; ++pr) {
        u32x4 a = ar[b][2 * pr], bb = ar[b][2 * pr + 1];
        afr[2 * pr] = u32x4{a[0], a[1], bb[0], bb[1]};
        afr[2 * pr + 1] = u32x4{a[2], a[3], bb[2], bb[3]};
      }
#pragma unroll
      for (int r = 0; r < 4; ++r)
#pragma unroll
        for (int c = 0; c < 4; ++c) acc[r][c] = mfma32(bfr[c], afr[r], acc[r][c]);
      SCHED;
      if constexpr (s + D < 10) { if (s + D < 8 || s + D < nst) loadA(IC<s + D>{}); }
      if constexpr (s == 4 || s == 5) { if (s + 4 < nst) { wait_lgkm0(); issueW2(p, lbase, w, s & 3, ks0 + s + 4, wvo, nt); } }
    }
  });
  STAMP(23);
#pragma unroll
  for (int r = 0; r < 4; ++r)
#pragma unroll
    for (int c = 0; c < 4; ++c)
      *(f32x4*)(lds + w * 16384 + (16 * r + li) * 256 + (((4 * c + g) ^ (li & 7)) * 16)) = acc[r][c];
  wait_lgkm0();
  bar();
  STAMP(24);
  const char* pa = lds + erow * 256 + (((2 * (tid & 7)) ^ (erow & 7)) * 16);
  const char* pb = lds + erow * 256 + (((2 * (tid & 7) + 1) ^ (erow & 7)) * 16);
  f32x4 sa = *(const f32x4*)(pa) + *(const f32x4*)(pa + 16384);
  f32x4 sb = *(const f32x4*)(pb) + *(const f32x4*)(pb + 16384);
  f32x4 ga = *(const f32x4*)(pa + 2 * 16384), gb = *(const f32x4*)(pb + 2 * 16384);
#pragma unroll
  for (int k = 3; k < 8; ++k) { ga += *(const f32x4*)(pa + k * 16384); gb += *(const f32x4*)(pb + k * 16384); }
  asm volatile("" : "+v"(xv));
  float sv[8] = {sa[0], sa[1], sa[2], sa[3], sb[0], sb[1], sb[2], sb[3]};
  float gv[8] = {ga[0], ga[1], ga[2], ga[3], gb[0], gb[1], gb[2], gb[3]};
  float ov[8];
#pragma unroll
  for (int e = 0; e < 8; ++e) {
    float d = rbf(gv[e]);
    float si = rbf(d * __builtin_amdgcn_rcpf(1.f + __builtin_amdgcn_exp2f(-1.44269504088896f * d)));
    float xx = (e & 1) ? hi2f(xv[e >> 1]) : lo2f(xv[e >> 1]);
    ov[e] = xx + rbf(rbf(sv[e]) * si);
  }
  u32x4 o; o[0] = pk2(ov[0], ov[1]); o[1] = pk2(ov[2], ov[3]); o[2] = pk2(ov[4], ov[5]); o[3] = pk2(ov[6], ov[7]);
  *(u32x4*)(p.out + (size_t)tok * 512 + nt * 64 + ecol) = o;
  if (MODE == 0 && bid < 256 && tid == 0 && nh == 0 && s8 == 0) __hip_atomic_store(epoch_p, tag, __ATOMIC_RELAXED, __HIP_MEMORY_SCOPE_AGENT);
  STAMP(25);
}

extern "C" void kernel_launch(void* const* d_in, const int* in_sizes, int n_in, void* d_out, int out_size, void* d_ws, size_t ws_size,
                              hipStream_t stream) {
  Params p;
  p.x = (const u16*)d_in[0]; p.wqkv = (const u16*)d_in[1]; p.wo = (const u16*)d_in[2];
  p.wg = (const u16*)d_in[4]; p.wu = (const u16*)d_in[5]; p.wd = (const u16*)d_in[6];
  p.out = (u16*)d_out; p.ws = (char*)d_ws;
  fused<0><<<256, 512, 0, stream>>>(p);
}
```

```cpp
#include <hip/hip_runtime.h>
#include <utility>
typedef unsigned short u16;
typedef unsigned long long u64;
typedef __attribute__((ext_vector_type(4))) short s16x4;
typedef __attribute__((ext_vector_type(4))) float f32x4;
typedef __attribute__((ext_vector_type(2))) float f32x2;
typedef __attribute__((ext_vector_type(2))) __bf16 bf16x2;
typedef __attribute__((ext_vector_type(8))) __bf16 bf16x8;
typedef __attribute__((ext_vector_type(2))) unsigned u32x2;
typedef __attribute__((ext_vector_type(4))) unsigned u32x4;
#define LAS __attribute__((address_space(3)))
#define GAS __attribute__((address_space(1)))
#define DI __device__ __forceinline__
#define SCHED __builtin_amdgcn_sched_barrier(0)

template <int... I, class F> DI void static_for_impl(std::integer_sequence<int, I...>, F&& f) { (f(std::integral_constant<int, I>{}), ...); }
template <int N, class F> DI void static_for(F&& f) { static_for_impl(std::make_integer_sequence<int, N>{}, f); }
template <int N> using IC = std::integral_constant<int, N>;

template <int N> DI void wait_vm() { asm volatile("s_waitcnt vmcnt(%0)" ::"i"(N) : "memory"); }
DI void wait_lgkm0() { asm volatile("s_waitcnt lgkmcnt(0)" ::: "memory"); }
DI void bar() { SCHED; __builtin_amdgcn_s_barrier(); SCHED; }
DI void dma16(unsigned lds_dst, unsigned voff, const void* sbase) {
  asm volatile("s_mov_b32 m0, %0\n\ts_nop 0\n\tglobal_load_lds_dwordx4 %1, %2" :: "s"(lds_dst), "v"(voff), "s"(sbase) : "memory");
}
DI void dma16_sc1(unsigned lds_dst, unsigned voff, const void* sbase) {
  asm volatile("s_mov_b32 m0, %0\n\ts_nop 0\n\tglobal_load_lds_dwordx4 %1, %2 sc1" :: "s"(lds_dst), "v"(voff), "s"(sbase) : "memory");
}
DI void gload_sc1(u32x4& d, unsigned voff, const void* sbase) {
  asm volatile("global_load_dwordx4 %0, %1, %2 sc1" : "=v"(d) : "v"(voff), "s"(sbase) : "memory");
}
template <int OFF> DI void gload(u32x4& d, unsigned voff, const void* sbase) {
  asm volatile("global_load_dwordx4 %0, %1, %2 offset:%3" : "=v"(d) : "v"(voff), "s"(sbase), "i"(OFF) : "memory");
}
DI void gstore_sc1(u32x4 d, unsigned voff, void* sbase) {
  asm volatile("global_store_dwordx4 %0, %1, %2 sc1\n\ts_nop 1" :: "v"(voff), "v"(d), "s"(sbase) : "memory");
}
DI f32x4 mfma32(u32x4 a, u32x4 b, f32x4 c) {
  return __builtin_amdgcn_mfma_f32_16x16x32_bf16(__builtin_bit_cast(bf16x8, a), __builtin_bit_cast(bf16x8, b), c, 0, 0, 0);
}
DI f32x4 mfma16(u32x2 a, u32x2 b, f32x4 c) {
  return __builtin_amdgcn_mfma_f32_16x16x16bf16_1k(__builtin_bit_cast(s16x4, a), __builtin_bit_cast(s16x4, b), c, 0, 0, 0);
}
DI f32x4 mfmaf(float a, float b) { return __builtin_amdgcn_mfma_f32_16x16x4f32(a, b, f32x4{0, 0, 0, 0}, 0, 0, 0); }
DI float dot2(unsigned a, unsigned b, float c) {
  return __builtin_amdgcn_fdot2_f32_bf16(__builtin_bit_cast(bf16x2, a), __builtin_bit_cast(bf16x2, b), c, false);
}
DI unsigned pk2(float lo, float hi) { bf16x2 c; c[0] = (__bf16)lo; c[1] = (__bf16)hi; return __builtin_bit_cast(unsigned, c); }
DI float lo2f(unsigned u) { return __uint_as_float(u << 16); }
DI float hi2f(unsigned u) { return __uint_as_float(u & 0xffff0000u); }
DI unsigned cvtpk(float lo, float hi) { unsigned r; asm("v_cvt_pk_bf16_f32 %0, %1, %2" : "=v"(r) : "v"(lo), "v"(hi)); return r; }
DI float rbf(float f) { unsigned r; asm("v_cvt_pk_bf16_f32 %0, 0, %1" : "=v"(r) : "v"(f)); return __uint_as_float(r); }
DI float vmax(float a, float b) { return __builtin_amdgcn_fmed3f(a, b, 3.0e38f); }
DI float relu(float a) { return __builtin_amdgcn_fmed3f(a, 0.f, 3.0e38f); }
DI u32x2 pk4(f32x4 v) { u32x2 r; r[0] = pk2(v[0], v[1]); r[1] = pk2(v[2], v[3]); return r; }
DI u32x4 cat(u32x2 a, u32x2 b) { u32x4 r; r[0] = a[0]; r[1] = a[1]; r[2] = b[0]; r[3] = b[1]; return r; }
DI float gsum(float v) {
  unsigned u = __float_as_uint(v);
  auto r = __builtin_amdgcn_permlane32_swap(u, u, false, false);
  v = __uint_as_float(r[0]) + __uint_as_float(r[1]);
  u = __float_as_uint(v);
  auto r2 = __builtin_amdgcn_permlane16_swap(u, u, false, false);
  return __uint_as_float(r2[0]) + __uint_as_float(r2[1]);
}
DI float gmax(float v) {
  unsigned u = __float_as_uint(v);
  auto r = __builtin_amdgcn_permlane32_swap(u, u, false, false);
  v = vmax(__uint_as_float(r[0]), __uint_as_float(r[1]));
  u = __float_as_uint(v);
  auto r2 = __builtin_amdgcn_permlane16_swap(u, u, false, false);
  return vmax(__uint_as_float(r2[0]), __uint_as_float(r2[1]));
}
DI u32x4 lds_b128(const char* lds, unsigned off) { return *(const u32x4*)(lds + off); }
DI u32x4 lds_tr2(const char* lds, unsigned off, unsigned step) {
  s16x4 t0 = __builtin_amdgcn_ds_read_tr16_b64_v4i16((LAS s16x4*)(lds + off));
  s16x4 t1 = __builtin_amdgcn_ds_read_tr16_b64_v4i16((LAS s16x4*)(lds + off + step));
  return cat(__builtin_bit_cast(u32x2, t0), __builtin_bit_cast(u32x2, t1));
}

#define S8ROT 0
constexpr int B_STAGE = 20480, NB = 5, A_STAGE = 18432;
constexpr int B_OFF = 0, A_OFF = NB * B_STAGE;
constexpr int T_END = A_OFF + 3 * A_STAGE;
constexpr int TR_OFF = T_END - 65536;
constexpr int PF_OFF = T_END;
constexpr int LDS_BYTES = PF_OFF + 256;
constexpr int NK16 = 144;
constexpr size_t SYNC_OFF = 16u << 20, XN_OFF = 20u << 20;
constexpr int XP = 1152;
struct Params { const u16 *x, *wqkv, *wo, *wg, *wu, *wd; u16* out; char* ws; };

#define STAMP(i)
#define STARG
#define STPASS

#define PH(i)
DI void mfma_acc(f32x4& c, u32x4 a, u32x4 b) {
  asm volatile("v_mfma_f32_16x16x32_bf16 %0, %1, %2, %0" : "+v"(c) : "v"(a), "v"(b));
}
DI void mfma_init(f32x4& c, u32x4 a, u32x4 b) {
  asm volatile("v_mfma_f32_16x16x32_bf16 %0, %1, %2, 0" : "=&v"(c) : "v"(a), "v"(b));
}
template <bool FIRST> DI void mfma_st(f32x4& c, u32x4 a, u32x4 b) { if constexpr (FIRST) mfma_init(c, a, b); else mfma_acc(c, a, b); }
DI float wave_sum(float v) {
  f32x4 a = mfmaf(v, 1.f);
  return mfmaf(1.f, (a[0] + a[1]) + (a[2] + a[3]))[0];
}
DI u32x4 ln_row(u32x4 x) {
  float v[8];
#pragma unroll
  for (int e = 0; e < 4; ++e) { v[2 * e] = lo2f(x[e]); v[2 * e + 1] = hi2f(x[e]); }
  const float x0 = __uint_as_float(__builtin_amdgcn_readfirstlane(__float_as_uint(v[0])));
  float s1 = 0, s2 = 0;
#pragma unroll
  for (int e = 0; e < 8; ++e) { float d = v[e] - x0; s1 += d; s2 += d * d; }
  float dm = wave_sum(s1) * (1.f / 512.f);
  float var = relu(wave_sum(s2) * (1.f / 512.f) - dm * dm);
  float mb = rbf(x0 + dm);
  float sd = __builtin_amdgcn_sqrtf(rbf(rbf(var) + 1e-5f));
  float inv = __builtin_amdgcn_rcpf(lo2f(pk2(sd, sd)));
  u32x4 o;
#pragma unroll
  for (int e = 0; e < 4; ++e) o[e] = cvtpk(rbf(v[2 * e] - mb) * inv, rbf(v[2 * e + 1] - mb) * inv);
  return o;
}
DI void issueW2(const Params& p, unsigned dst, int ks, unsigned wvo, int nt) {
  const char* wb = ((ks < 16) ? (const char*)p.wo + (size_t)ks * 32768 : (const char*)p.wd + (size_t)(ks - 16) * 32768) + nt * 128;
#pragma unroll
  for (int i = 0; i < 4; ++i) dma16(dst + i * 1024, wvo, wb + i * 8192);
}
DI void poll_flags(GAS u64* f, u64 tag, unsigned need, int li) {
  const bool skip = !((need >> li) & 1);
#pragma unroll 1
  for (unsigned spins = 0; spins < (1u << 20); ++spins) {
    u64 v = __hip_atomic_load(f + li, __ATOMIC_RELAXED, __HIP_MEMORY_SCOPE_AGENT);
    if (__all(skip || v == tag)) break;
    __builtin_amdgcn_s_sleep(2);
  }
}
constexpr int ngrp(int t, bool g1, bool isq) {
  if (t < 0) return 0;
  int n = 0;
  if (t + 5 < 16) n += g1 ? 2 : 3;
  if (t >= 1 && t <= 10) n += 1 + ((isq && g1 && (t & 1)) ? 1 : 0);
  return n;
}

template <bool ISQ>
DI void phase1(const Params& p, char* lds, int mt, int j, int rot, int ks0, unsigned wvo, int nt, GAS u64* flags, u64& tag,
               u32x4 (&breg)[4][4], const unsigned (&trl)[4] STARG) {
  const int tid = threadIdx.x, lane = tid & 63;
  const int w = __builtin_amdgcn_readfirstlane(tid >> 6), G1 = w >> 2, wm = G1, wn = w & 3;
  const int li = lane & 15, g = lane >> 4;
  const unsigned lbase = (unsigned)(size_t)lds;
  const int T0 = mt * 128;
  const unsigned lo16 = lane * 16;
  char* xn = p.ws + XN_OFF + (size_t)mt * 144 * XP;
  u32x4 xr, xh; u64 ep;
  asm volatile("global_load_dwordx2 %0, %1, %2 sc1" : "=v"(ep) : "v"(0u), "s"(flags - 128) : "memory");
  { int rho = 8 * j + w; int tok = T0 + 32 * (rho >> 5) + 2 * (rho & 15) + ((rho >> 4) & 1);
    gload<0>(xr, lo16, (const char*)p.x + (size_t)tok * 1024);
    int th = (mt ? T0 - 16 : 0) + 2 * (j & 7) + (j >> 3);
    gload<0>(xh, lo16, (const char*)p.x + (size_t)(w == 0 ? th : tok) * 1024); }
  const unsigned trw = lbase + TR_OFF + w * 4096;
  issueW2(p, trw, ks0, wvo, nt);
  issueW2(p, trw + 32768, ks0 + 1, wvo, nt);
  STAMP(21);
  int fa = 0, fx = 0;
  if (!ISQ) { int i = j - 8, f0 = 512 + 160 * i; if (i & 1) { fx = f0; fa = f0 + 32; } else { fa = f0; fx = f0 + 128; } }
  const char *sbA, *sbB, *sbC;
  unsigned voA, voB, voC, vx0, vx1, vxh;
  constexpr unsigned ldA = ISQ ? 3072 : 3584, ldC = 3584;
  const unsigned ldB = (ISQ && !G1) ? 3072 : 3584;
  {
    int q = lane >> 3, slot = lane & 7, s = ((q >> 1) & 1) | ((wn & 1) << 1);
    unsigned co = 16 * (2 * ((slot >> 1) ^ s) + (slot & 1));
    voA = (8 * wn + q) * ldA + co; voB = (8 * wn + q) * ldB + co;
    if (ISQ) {
      sbA = (const char*)(p.wqkv + G1 * 512 + 64 * j);
      sbB = G1 ? (const char*)(p.wg + 64 * j) : (const char*)(p.wqkv + 1024 + 64 * j);
      sbC = (const char*)(p.wu + 64 * j);
      voC = (8 * wn + q) * ldC + co;
    } else {
      sbA = (const char*)(p.wg + fa + 64 * G1);
      sbB = (const char*)(p.wu + fa + 64 * G1);
      sbC = (const char*)(((wn & 1) ? p.wu : p.wg) + fx);
      int r16 = lane >> 2, cp = lane & 3, sg = ((r16 >> 3) & 1) | ((wn & 1) << 1);
      voC = (16 * (wn >> 1) + r16) * ldC + 16 * (cp ^ sg);
    }
    vx0 = (16 * w + q) * XP + 16 * (slot ^ (q >> 1));
    vx1 = (16 * w + 8 + q) * XP + 16 * (slot ^ (4 + (q >> 1)));
    { int rho = 128 + 4 * wn + (q & 3); vxh = rho * XP + 16 * (slot ^ ((rho >> 1) & 7)); }
  }
  const unsigned dB = lbase + B_OFF + G1 * 4096 + wn * 1024;
  auto issueB = [&](int t) {
    unsigned dst = dB + ((t + 4) % NB) * B_STAGE;
    unsigned kr = 32 * ((t + rot) & 15);
    dma16(dst, voA, sbA + kr * ldA);
    dma16(dst + 8192, voB, sbB + kr * ldB);
    if (!G1) dma16(dst + 16384, voC, sbC + kr * ldC);
  };
  auto issueA = [&](int s, int half) {
    unsigned dst = lbase + A_OFF + (s % 3) * A_STAGE;
    const char* src = xn + 64 * ((2 * s + rot) & 15);
    dma16_sc1(dst + (2 * w + half) * 1024, half ? vx1 : vx0, src);
    if (ISQ && half == 0) { if (G1) { if (lane < 32) dma16_sc1(dst + 16384 + wn * 512, vxh, src); } }
  };
  wait_vm<8>();
  asm volatile("" : "+v"(xr), "+v"(xh), "+v"(ep));
  STAMP(22);
  tag = (((u64)(unsigned)__builtin_amdgcn_readfirstlane((unsigned)(ep >> 32)) << 32) | (unsigned)__builtin_amdgcn_readfirstlane((unsigned)ep)) + 1;
  if (w == 0) {
    u32x4 o0 = ln_row(xr), o1 = ln_row(xh);
    gstore_sc1(o0, lo16, xn + (size_t)(8 * j) * XP);
    gstore_sc1(o1, lo16, xn + (size_t)(128 + j) * XP);
  } else gstore_sc1(ln_row(xr), lo16, xn + (size_t)(8 * j + w) * XP);
  STAMP(23);
  issueB(1); issueB(2);
  if (G1) wait_vm<4>(); else wait_vm<6>();
  bar();
  if (tid < 16) __hip_atomic_store(flags + tid * 16 + j, tag, __ATOMIC_RELAXED, __HIP_MEMORY_SCOPE_AGENT);
  STAMP(1);
  auto readW = [&](u32x4 (&d)[4], unsigned off) {
#pragma unroll
    for (int c = 0; c < 4; ++c) d[c] = lds_tr2(lds, trl[c] + off, 2048);
  };
  readW(breg[0], 0); readW(breg[1], 32768);
  wait_lgkm0();
#pragma unroll
  for (int c = 0; c < 4; ++c) asm volatile("" : "+v"(breg[0][c]), "+v"(breg[1][c]));
  issueW2(p, trw, ks0 + 2, wvo, nt);
  issueW2(p, trw + 32768, ks0 + 3, wvo, nt);
  issueB(3); issueB(4);
  unsigned aoff[2], hoff[2], boff[2][3];
  { int rho = 64 * wm + li; int f = (rho >> 1) & 7; aoff[0] = A_OFF + rho * 128 + 16 * (g ^ f); aoff[1] = A_OFF + rho * 128 + 16 * ((4 + g) ^ f); }
  { int rho = wm ? (40 + (li & 7) + 16 * (li >> 3)) : 128 + li; int f = (rho >> 1) & 7; hoff[0] = A_OFF + rho * 128 + 16 * (g ^ f); hoff[1] = A_OFF + rho * 128 + 16 * ((4 + g) ^ f); }
  { int q = (lane >> 2) & 3, pp = lane & 3, s = ((q >> 1) & 1) | ((g & 1) << 1);
    unsigned rb = B_OFF + g * 1024 + 128 * q + 8 * pp;
    if (ISQ) {
      boff[0][0] = rb + 32 * (wn ^ s); boff[0][1] = boff[0][2] = 0;
    } else {
      int p0 = wn >> 1, j0 = 2 * (wn & 1);
      boff[0][0] = rb + p0 * 4096 + 32 * (j0 ^ s); boff[0][1] = rb + p0 * 4096 + 32 * ((j0 + 1) ^ s);
      int up = pp >> 1, d = 2 * (g >> 1) + up, r16 = 8 * (g & 1) + q, sg = (g & 1) | (up << 1);
      boff[0][2] = B_OFF + 16384 + d * 1024 + 64 * r16 + 16 * (wn ^ sg) + 8 * (pp & 1);
    }
#pragma unroll
    for (int c = 0; c < (ISQ ? 1 : 3); ++c) { boff[1][c] = boff[0][c] + 3 * B_STAGE; asm volatile("" : "+v"(boff[0][c]), "+v"(boff[1][c])); }
  }
  asm volatile("" : "+v"(aoff[0]), "+v"(aoff[1]));
  if (ISQ) asm volatile("" : "+v"(hoff[0]), "+v"(hoff[1]));
  f32x4 bprev, bh[2]; bool cv[4];
  auto make_masks = [&]() {
    const float NEG = -1e30f;
    const bool nohalo = (mt == 0) && (wm == 0);
    int ln = lane; asm volatile("" : "+v"(ln));
    const int d0 = (ln & 15) - 4 * (ln >> 4);
#pragma unroll
    for (int e = 0; e < 4; ++e) {
      int d = d0 - e;
      cv[e] = (unsigned)d <= 7u;
      bprev[e] = (d <= -9) ? 0.f : NEG;
      bh[0][e] = (d < 0 && ln < 32 && !nohalo) ? 0.f : NEG;
      bh[1][e] = nohalo ? NEG : bprev[e];
    }
  };
  f32x4 acc[4][5]; f32x4 acch[2];
  if (G1) wait_vm<4>(); else wait_vm<6>();
  readW(breg[2], 0); readW(breg[3], 32768);
  wait_lgkm0();
#pragma unroll
  for (int c = 0; c < 4; ++c) asm volatile("" : "+v"(breg[2][c]), "+v"(breg[3][c]));
  STAMP(2);
  poll_flags(flags + j * 16, tag, 0xffffu, li);
  STAMP(3);
  bar();
  issueA(0, 0); issueA(0, 1); issueB(0); issueA(1, 0); issueA(1, 1); issueA(2, 0); issueA(2, 1);
  if (ISQ && G1) wait_vm<6>(); else wait_vm<4>();
  bar();
  STAMP(4);
  u32x4 af[2][4], ah[2], bf[5];
  auto rdB = [&](auto tc, auto cc) {
    constexpr int t = tc, c = cc;
    constexpr int sl = (t + 4) % NB, bh_ = sl >= 3, bo = (sl - 3 * bh_) * B_STAGE;
    if (ISQ) bf[c] = lds_tr2(lds, boff[bh_][0] + bo + c * 4096, 512);
    else if (c < 4) bf[c] = lds_tr2(lds, boff[bh_][c & 1] + bo + (c >> 1) * 8192, 512);
    else bf[c] = lds_tr2(lds, boff[bh_][2] + bo, 256);
  };
  auto rdA = [&](auto tc, auto rc) {
    constexpr int t = tc, r = rc, P = t & 1, ao = ((t >> 1) % 3) * A_STAGE;
    if constexpr (r < 4) af[P][r] = lds_b128(lds, aoff[P] + ao + r * 2048);
    else if (ISQ) ah[P] = lds_b128(lds, hoff[P] + ao);
  };
  static_for<5>([&](auto c) { rdA(IC<0>{}, c); rdB(IC<0>{}, c); });
#define PRIO_MODE 1
  if (PRIO_MODE == 1) { if (!G1) __builtin_amdgcn_s_setprio(1); }
  if (PRIO_MODE == 2) { if (G1) __builtin_amdgcn_s_setprio(1); }
  static_for<16>([&](auto tc) {
    constexpr int t = tc; constexpr int P = t & 1;
    constexpr int hq = ISQ ? 3 : 2;
    constexpr int n0 = t >= 12 ? 0 : (t == 0 ? 4 : (t == 1 ? 2 + ngrp(0, false, ISQ) : ngrp(t - 2, false, ISQ) + ngrp(t - 1, false, ISQ)));
    constexpr int n1 = t >= 12 ? 0 : (t == 0 ? 2 * hq : (t == 1 ? hq + ngrp(0, true, ISQ) : ngrp(t - 2, true, ISQ) + ngrp(t - 1, true, ISQ)));
    static_for<5>([&](auto cc) {
      constexpr int c = cc;
#pragma unroll
      for (int r = 0; r < 4; ++r) {
        if (ISQ && c == 2) mfma_st<t == 0>(acc[r][c], af[P][r], bf[c]);
        else mfma_st<t == 0>(acc[r][c], bf[c], af[P][r]);
      }
      if (ISQ && c == 1) mfma_st<t == 0>(acch[0], bf[1], ah[P]);
      if (ISQ && c == 2) mfma_st<t == 0>(acch[1], ah[P], bf[2]);
      SCHED;
      if constexpr (c == 0) {
        if constexpr (t <= 12) { if (G1) wait_vm<n1>(); else wait_vm<n0>(); }
        wait_lgkm0();
        bar();
      }
      if constexpr (t < 15) {
        rdB(IC<t + 1>{}, cc);
        if constexpr (c == 0) { rdA(IC<t + 1>{}, IC<0>{}); rdA(IC<t + 1>{}, IC<1>{}); }
        if constexpr (c == 1) { rdA(IC<t + 1>{}, IC<2>{}); rdA(IC<t + 1>{}, IC<3>{}); }
        if constexpr (c == 2) rdA(IC<t + 1>{}, IC<4>{});
      }
      if constexpr (c == 1 && t + 5 < 16) issueB(t + 5);
      if constexpr (c == 2 && t >= 1 && t <= 10) issueA((t - 1) / 2 + 3, (t - 1) & 1);
      if constexpr (c == 1 && t == 12) issueW2(p, lbase + w * 4096, ks0 + 4, wvo, nt);
      if constexpr (c == 1 && t == 14) issueW2(p, lbase + 32768 + w * 4096, ks0 + 5, wvo, nt);
      if constexpr (c == 1 && t == 15) issueW2(p, lbase + 65536 + w * 4096, ks0 + 6, wvo, nt);
      if constexpr (c == 3 && t == 15) issueW2(p, lbase + 98304 + w * 4096, ks0 + 7, wvo, nt);
      SCHED;
    });
  });
  __builtin_amdgcn_s_setprio(0);
  asm volatile("s_nop 15\n\ts_nop 7" ::: "memory");
  SCHED;
  STAMP(5);
  char* a2b = p.ws + (size_t)(4 * mt + 2 * wm) * NK16 * 1024;
  auto reglu = [&](f32x4 ga, f32x4 ua) {
    u32x2 r; r[0] = cvtpk(relu(ga[0]) * ua[0], relu(ga[1]) * ua[1]); r[1] = cvtpk(relu(ga[2]) * ua[2], relu(ga[3]) * ua[3]); return r;
  };
  if constexpr (ISQ) {
    {
      u32x2 hv[4];
#pragma unroll
      for (int r = 0; r < 4; ++r) hv[r] = reglu(acc[r][3], acc[r][4]);
      char* pb = a2b + (size_t)(32 + 4 * j + wn) * 1024;
      gstore_sc1(cat(hv[0], hv[1]), lo16, pb);
      gstore_sc1(cat(hv[2], hv[3]), lo16, pb + NK16 * 1024);
    }
    make_masks();
    u32x2 Qp[4], Kp[4], Vp[4], Kh, Vh;
#pragma unroll
    for (int r = 0; r < 4; ++r) { Qp[r] = pk4(acc[r][0]); Kp[r] = pk4(acc[r][1]); Vp[r] = pk4(acc[r][2]); }
    Kh = pk4(acch[0]); Vh = pk4(acch[1]);
    const float SC = 0.25f * 1.44269504088896f;
    const unsigned mc0 = (cv[0] ? 0xffffu : 0u) | (cv[1] ? 0xffff0000u : 0u);
    const unsigned mc1 = (cv[2] ? 0xffffu : 0u) | (cv[3] ? 0xffff0000u : 0u);
    float l[4][4], mx[4];
#pragma unroll
    for (int r = 0; r < 4; ++r) {
      f32x4 sc = mfma16(Kp[r], Qp[r], f32x4{0, 0, 0, 0});
      f32x4 sp = (r >= 2) ? mfma16(Kp[r - 2], Qp[r], bprev) : mfma16(Kh, Qp[r], bh[r & 1]);
#pragma unroll
      for (int e = 0; e < 4; ++e) l[r][e] = rbf(cv[e] ? sc[e] : sp[e]);
      mx[r] = vmax(vmax(l[r][0], l[r][1]), vmax(l[r][2], l[r][3]));
    }
    u32x2 ov[4];
#pragma unroll
    for (int r = 0; r < 4; ++r) mx[r] = gmax(mx[r]) * SC;
#pragma unroll
    for (int r = 0; r < 4; ++r) {
#pragma unroll
      for (int e = 0; e < 4; ++e) l[r][e] = __builtin_amdgcn_exp2f(l[r][e] * SC - mx[r]);
      float sum = mfmaf(1.f, (l[r][0] + l[r][1]) + (l[r][2] + l[r][3]))[0];
      float rs = __builtin_amdgcn_rcpf(sum);
      unsigned p0 = cvtpk(l[r][0] * rs, l[r][1] * rs), p1 = cvtpk(l[r][2] * rs, l[r][3] * rs);
      u32x4 pb; pb[0] = p0 & ~mc0; pb[1] = p1 & ~mc1; pb[2] = p0 & mc0; pb[3] = p1 & mc1;
      f32x4 o = mfma32(cat((r >= 2) ? Vp[r - 2] : Vh, Vp[r]), pb, f32x4{0, 0, 0, 0});
      ov[r] = pk4(o);
    }
    char* pb = a2b + (size_t)(4 * j + wn) * 1024;
    gstore_sc1(cat(ov[0], ov[1]), lo16, pb);
    gstore_sc1(cat(ov[2], ov[3]), lo16, pb + NK16 * 1024);
  } else {
    char* pb = a2b + (size_t)(32 + (fa >> 4) + 2 * wn) * 1024;
#pragma unroll
    for (int c = 0; c < 2; ++c) {
      u32x2 hv[4];
#pragma unroll
      for (int r = 0; r < 4; ++r) hv[r] = reglu(acc[r][c], acc[r][2 + c]);
      gstore_sc1(cat(hv[0], hv[1]), lo16, pb + c * 1024);
      gstore_sc1(cat(hv[2], hv[3]), lo16, pb + c * 1024 + NK16 * 1024);
    }
    u32x2 hq[2];
#pragma unroll
    for (int rp = 0; rp < 2; ++rp) {
      f32x4 gt, up;
#pragma unroll
      for (int e = 0; e < 4; ++e) {
        auto r = __builtin_amdgcn_permlane32_swap(__float_as_uint(acc[rp][4][e]), __float_as_uint(acc[rp + 2][4][e]), false, false);
        gt[e] = __uint_as_float(r[0]); up[e] = __uint_as_float(r[1]);
      }
      hq[rp] = reglu(gt, up);
    }
    unsigned vo = (unsigned)((g >> 1) * NK16 * 1024 + ((2 * (wn & 1) + (g & 1)) * 16 + li) * 16);
    gstore_sc1(cat(hq[0], hq[1]), vo, a2b + (size_t)(32 + ((fx + 8 * wn) >> 4)) * 1024);
  }
}

__global__ __launch_bounds__(512, 2) void fused(Params p) {
  __shared__ __attribute__((aligned(16))) char lds[LDS_BYTES];
  const int tid = threadIdx.x, lane = tid & 63, w = __builtin_amdgcn_readfirstlane(tid >> 6);
  const int li = lane & 15, g = lane >> 4;
  const int bid = blockIdx.x, xcd = bid & 7, idx = bid >> 3;
  const int mt = (xcd & 3) * 4 + (idx >> 3), s8 = (idx + S8ROT) & 7, nh = xcd >> 2;
  const int nt = s8;
  const unsigned lbase = (unsigned)(size_t)lds;
  STAMP(0);
  {
    u64 pc; int len;
    asm volatile("s_getpc_b64 %0\n.Lfused_pf_begin:\n\ts_mov_b32 %1, .Lfused_pf_end-.Lfused_pf_begin" : "=s"(pc), "=s"(len));
    int off = s8 * 8192 + lane * 128;
    off = off < len ? off : 0;
    if (w == 7) asm volatile("s_mov_b32 m0, %0\n\ts_nop 0\n\tglobal_load_lds_dword %1, %2" :: "s"(lbase + PF_OFF), "v"(off), "s"(pc) : "memory");
  }
  const int ks0 = 9 * w;
  unsigned wvo;
  { int q = lane >> 3, slot = lane & 7; wvo = q * 1024 + 16 * (2 * ((slot >> 1) ^ ((q >> 1) & 3)) + (slot & 1)); }
  GAS u64* epoch_p = (GAS u64*)(p.ws + SYNC_OFF + mt * 8192);
  GAS u64* flags0 = epoch_p + 128;
  GAS u64* flags1 = epoch_p + 512;
  u64 tag;
  const int j = s8 < 4 ? 4 * nh + s8 : 8 + 4 * nh + (s8 - 4);
  const int rot = 2 * ((s8 + 2 * (idx >> 3)) & 7);
  unsigned trl[4];
  { int q = (lane >> 2) & 3, pp = lane & 3, qq = 4 * (g & 1) + q;
#pragma unroll
    for (int c = 0; c < 4; ++c) { trl[c] = TR_OFF + w * 4096 + (g >> 1) * 1024 + 128 * qq + 32 * (c ^ ((qq >> 1) & 3)) + 8 * pp; asm volatile("" : "+v"(trl[c])); } }
  u32x4 breg[4][4];
  if (s8 < 4) phase1<true>(p, lds, mt, j, rot, ks0, wvo, nt, flags0, tag, breg, trl STPASS);
  else phase1<false>(p, lds, mt, j, rot, ks0, wvo, nt, flags0, tag, breg, trl STPASS);
  STAMP(6);
  const int erow = tid >> 3, ecol = (tid & 7) * 8;
  const int rho = 128 * mt + 64 * nh + erow;
  const int tok = 32 * (rho >> 5) + 2 * (rho & 15) + ((rho >> 4) & 1);
  u32x4 xv;
  gload<0>(xv, (unsigned)(tok * 1024 + nt * 128 + ecol * 2), p.x);
  wait_vm<1>();
  bar();
  if (tid < 16) __hip_atomic_store(flags1 + tid * 16 + j, tag, __ATOMIC_RELAXED, __HIP_MEMORY_SCOPE_AGENT);
  STAMP(7);
  unsigned tro[2][4];
#pragma unroll
  for (int c = 0; c < 4; ++c) {
    int ln = lane; asm volatile("" : "+v"(ln));
    int q = (ln >> 2) & 3, pp = ln & 3, gg = ln >> 4, qq = 4 * (gg & 1) + q;
    tro[0][c] = w * 4096 + (gg >> 1) * 1024 + 128 * qq + 32 * (c ^ ((qq >> 1) & 3)) + 8 * pp;
    tro[1][c] = tro[0][c] + 65536; asm volatile("" : "+v"(tro[0][c]), "+v"(tro[1][c]));
  }
  poll_flags(flags1 + j * 16, tag, (unsigned)((w < 4 ? 0x01E0003E00F1001Full : 0xC00070001C000700ull) >> (16 * (w & 3))) & 0xffffu, li);
  STAMP(8);
  const char* ab = p.ws + ((size_t)(4 * mt + 2 * nh) * NK16 + 2 * ks0) * 1024;
  const unsigned lo16 = lane * 16;
  u32x4 ar[9][4];
  f32x4 acc[4][4];
#pragma unroll
  for (int r = 0; r < 4; ++r)
#pragma unroll
    for (int c = 0; c < 4; ++c) acc[r][c] = f32x4{0, 0, 0, 0};
  auto loadA = [&](auto sc) {
    constexpr int s = sc;
    const char* a0 = ab + s * 2048;
    gload_sc1(ar[s][0], lo16, a0);
    gload_sc1(ar[s][1], lo16, a0 + 1024);
    gload_sc1(ar[s][2], lo16, a0 + NK16 * 1024);
    gload_sc1(ar[s][3], lo16, a0 + NK16 * 1024 + 1024);
  };
  auto flush = [&](unsigned base, unsigned rstride) {
#pragma unroll
    for (int r = 0; r < 4; ++r)
#pragma unroll
      for (int c = 0; c < 4; ++c)
        *(f32x4*)(lds + base + r * rstride + li * 256 + (((4 * c + g) ^ (li & 7)) * 16)) = acc[r][c];
  };
  static_for<4>([&](auto sc) { loadA(sc); });
  static_for<9>([&](auto sc) {
    constexpr int s = sc;
    constexpr int N = s <= 4 ? 12 : (s == 5 ? 16 : (s == 6 ? 12 : (s == 7 ? 8 : 0)));
    if constexpr (s == 7) {
      if (w == 1) {
        flush(131072, 4096);
#pragma unroll
        for (int r = 0; r < 4; ++r)
#pragma unroll
          for (int c = 0; c < 4; ++c) acc[r][c] = f32x4{0, 0, 0, 0};
      }
    }
    wait_vm<N>();
    asm volatile("" : "+v"(ar[s][0]), "+v"(ar[s][1]), "+v"(ar[s][2]), "+v"(ar[s][3]));
    u32x4 bfr[4];
#pragma unroll
    for (int c = 0; c < 4; ++c) {
      if constexpr (s < 4) bfr[c] = breg[s][c];
      else bfr[c] = lds_tr2(lds, tro[(s >> 1) & 1][c] + (s & 1) * 32768, 2048);
    }
    u32x4 afr[4];
#pragma unroll
    for (int pr = 0; pr < 2; ++pr) {
      u32x4 a = ar[s][2 * pr], bb = ar[s][2 * pr + 1];
      afr[2 * pr] = u32x4{a[0], a[1], bb[0], bb[1]};
      afr[2 * pr + 1] = u32x4{a[2], a[3], bb[2], bb[3]};
    }
#pragma unroll
    for (int r = 0; r < 4; ++r)
#pragma unroll
      for (int c = 0; c < 4; ++c) acc[r][c] = mfma32(bfr[c], afr[r], acc[r][c]);
    SCHED;
    if constexpr (s <= 4) loadA(IC<s + 4>{});
    if constexpr (s == 4) { wait_lgkm0(); issueW2(p, lbase + w * 4096, ks0 + 8, wvo, nt); }
    STAMP(9 + s);
  });
  flush(w * 4096, 32768);
  wait_lgkm0();
  STAMP(18);
  bar();
  STAMP(19);
  const char* pa = lds + (erow >> 4) * 32768 + (erow & 15) * 256 + (((2 * (tid & 7)) ^ (erow & 7)) * 16);
  const char* pb = lds + (erow >> 4) * 32768 + (erow & 15) * 256 + (((2 * (tid & 7) + 1) ^ (erow & 7)) * 16);
  const int xo2 = 131072 - (erow >> 4) * (32768 - 4096);
  f32x4 sa = *(const f32x4*)(pa) + *(const f32x4*)(pa + xo2);
  f32x4 sb = *(const f32x4*)(pb) + *(const f32x4*)(pb + xo2);
  f32x4 ga = *(const f32x4*)(pa + 4096), gb = *(const f32x4*)(pb + 4096);
#pragma unroll
  for (int k = 2; k < 8; ++k) { ga += *(const f32x4*)(pa + k * 4096); gb += *(const f32x4*)(pb + k * 4096); }
  asm volatile("" : "+v"(xv));
  float sv[8] = {sa[0], sa[1], sa[2], sa[3], sb[0], sb[1], sb[2], sb[3]};
  float gv[8] = {ga[0], ga[1], ga[2], ga[3], gb[0], gb[1], gb[2], gb[3]};
  float ov[8];
#pragma unroll
  for (int e = 0; e < 8; ++e) {
    float d = rbf(gv[e]);
    float si = rbf(d * __builtin_amdgcn_rcpf(1.f + __builtin_amdgcn_exp2f(-1.44269504088896f * d)));
    float xx = (e & 1) ? hi2f(xv[e >> 1]) : lo2f(xv[e >> 1]);
    ov[e] = xx + rbf(rbf(sv[e]) * si);
  }
  u32x4 o; o[0] = pk2(ov[0], ov[1]); o[1] = pk2(ov[2], ov[3]); o[2] = pk2(ov[4], ov[5]); o[3] = pk2(ov[6], ov[7]);
  *(u32x4*)(p.out + (size_t)tok * 512 + nt * 64 + ecol) = o;
  if (tid == 0 && j == 0) __hip_atomic_store(epoch_p, tag, __ATOMIC_RELAXED, __HIP_MEMORY_SCOPE_AGENT);
  asm volatile(".Lfused_pf_end:");
}

extern "C" void kernel_launch(void* const* d_in, const int* in_sizes, int n_in, void* d_out, int out_size, void* d_ws, size_t ws_size,
                              hipStream_t stream) {
  Params p;
  p.x = (const u16*)d_in[0]; p.wqkv = (const u16*)d_in[1]; p.wo = (const u16*)d_in[2];
  p.wg = (const u16*)d_in[4]; p.wu = (const u16*)d_in[5]; p.wd = (const u16*)d_in[6];
  p.out = (u16*)d_out; p.ws = (char*)d_ws;
  fused<<<256, 512, 0, stream>>>(p);
}
```

```cpp
#include <hip/hip_runtime.h>
#include <utility>
typedef unsigned short u16;
typedef unsigned long long u64;
typedef __attribute__((ext_vector_type(4))) short s16x4;
typedef __attribute__((ext_vector_type(4))) float f32x4;
typedef __attribute__((ext_vector_type(2))) float f32x2;
typedef __attribute__((ext_vector_type(2))) __bf16 bf16x2;
typedef __attribute__((ext_vector_type(8))) __bf16 bf16x8;
typedef __attribute__((ext_vector_type(2))) unsigned u32x2;
typedef __attribute__((ext_vector_type(4))) unsigned u32x4;
#define LAS __attribute__((address_space(3)))
#define GAS __attribute__((address_space(1)))
#define DI __device__ __forceinline__
#define SCHED __builtin_amdgcn_sched_barrier(0)
#define HZ "s_nop 4\n\t"

template <int... I, class F> DI void static_for_impl(std::integer_sequence<int, I...>, F&& f) { (f(std::integral_constant<int, I>{}), ...); }
template <int N, class F> DI void static_for(F&& f) { static_for_impl(std::make_integer_sequence<int, N>{}, f); }
template <int N> using IC = std::integral_constant<int, N>;

template <int N> DI void wait_vm() { asm volatile("s_waitcnt vmcnt(%0)" ::"i"(N) : "memory"); }
DI void wait_lgkm0() { __builtin_amdgcn_s_waitcnt(0xC07F); asm volatile("" ::: "memory"); }
DI void bar() { SCHED; __builtin_amdgcn_s_barrier(); SCHED; }
DI void dma16(unsigned lds_dst, unsigned voff, const void* sbase) {
  asm volatile("s_mov_b32 m0, %0\n\ts_nop 3\n\tglobal_load_lds_dwordx4 %1, %2" :: "s"(lds_dst), "v"(voff), "s"(sbase) : "memory");
}
DI void dma16_sc1(unsigned lds_dst, unsigned voff, const void* sbase) {
  asm volatile("s_mov_b32 m0, %0\n\ts_nop 3\n\tglobal_load_lds_dwordx4 %1, %2 sc1" :: "s"(lds_dst), "v"(voff), "s"(sbase) : "memory");
}
template <int OFF> DI void gload(u32x4& d, unsigned voff, const void* sbase) {
  asm volatile(HZ "global_load_dwordx4 %0, %1, %2 offset:%3" : "=v"(d) : "v"(voff), "s"(sbase), "i"(OFF) : "memory");
}
DI void gload8_sc1(u64& d, unsigned voff, const void* sbase) {
  asm volatile(HZ "global_load_dwordx2 %0, %1, %2 sc1" : "=v"(d) : "v"(voff), "s"(sbase) : "memory");
}
DI void pin64(u64& x) { asm volatile("" : "+v"(x)); }
DI void gstore_sc1(u32x4 d, unsigned voff, void* sbase) {
  asm volatile(HZ "global_store_dwordx4 %0, %1, %2 sc1\n\ts_nop 1" :: "v"(voff), "v"(d), "s"(sbase) : "memory");
}
DI f32x4 mfma32(u32x4 a, u32x4 b, f32x4 c) {
  return __builtin_amdgcn_mfma_f32_16x16x32_bf16(__builtin_bit_cast(bf16x8, a), __builtin_bit_cast(bf16x8, b), c, 0, 0, 0);
}
DI f32x4 mfma16(u32x2 a, u32x2 b, f32x4 c) {
  return __builtin_amdgcn_mfma_f32_16x16x16bf16_1k(__builtin_bit_cast(s16x4, a), __builtin_bit_cast(s16x4, b), c, 0, 0, 0);
}
DI f32x4 mfmaf(float a, float b) { return __builtin_amdgcn_mfma_f32_16x16x4f32(a, b, f32x4{0, 0, 0, 0}, 0, 0, 0); }
DI unsigned pk2(float lo, float hi) { bf16x2 c; c[0] = (__bf16)lo; c[1] = (__bf16)hi; return __builtin_bit_cast(unsigned, c); }
DI float lo2f(unsigned u) { return __uint_as_float(u << 16); }
DI float hi2f(unsigned u) { return __uint_as_float(u & 0xffff0000u); }
DI unsigned cvtpk(float lo, float hi) { unsigned r; asm("v_cvt_pk_bf16_f32 %0, %1, %2" : "=v"(r) : "v"(lo), "v"(hi)); return r; }
DI float rbf(float f) { unsigned r; asm("v_cvt_pk_bf16_f32 %0, 0, %1" : "=v"(r) : "v"(f)); return __uint_as_float(r); }
DI float vmax(float a, float b) { return __builtin_amdgcn_fmed3f(a, b, 3.0e38f); }
DI float relu(float a) { return __builtin_amdgcn_fmed3f(a, 0.f, 3.0e38f); }
DI u32x2 pk4(f32x4 v) { u32x2 r; r[0] = pk2(v[0], v[1]); r[1] = pk2(v[2], v[3]); return r; }
DI u32x4 cat(u32x2 a, u32x2 b) { u32x4 r; r[0] = a[0]; r[1] = a[1]; r[2] = b[0]; r[3] = b[1]; return r; }
DI float gmax(float v) {
  unsigned u = __float_as_uint(v);
  auto r = __builtin_amdgcn_permlane32_swap(u, u, false, false);
  v = vmax(__uint_as_float(r[0]), __uint_as_float(r[1]));
  u = __float_as_uint(v);
  auto r2 = __builtin_amdgcn_permlane16_swap(u, u, false, false);
  return vmax(__uint_as_float(r2[0]), __uint_as_float(r2[1]));
}
DI u32x4 lds_b128(const char* lds, unsigned off) { return *(const u32x4*)(lds + off); }
DI u32x4 lds_tr2(const char* lds, unsigned off, unsigned step) {
  s16x4 t0 = __builtin_amdgcn_ds_read_tr16_b64_v4i16((LAS s16x4*)(lds + off));
  s16x4 t1 = __builtin_amdgcn_ds_read_tr16_b64_v4i16((LAS s16x4*)(lds + off + step));
  return cat(__builtin_bit_cast(u32x2, t0), __builtin_bit_cast(u32x2, t1));
}

constexpr int NK16 = 144;
constexpr size_t GRP = 786432, XN_OFF = 589824, ST_OFF = 755712, SYNC_OFF = 757760;
constexpr int XP = 1152;
constexpr int CS_OFF = 157696;
constexpr int SL_OFF = CS_OFF + 1280;
constexpr int RM_OFF = SL_OFF + 1024;
#define NW2_ 20
constexpr int NW2 = NW2_, NA2 = 36 - NW2_, A2_OFF = NW2 * 4096;
constexpr int WU_OFF = RM_OFF + 32;
constexpr int LDS_BYTES = WU_OFF + 1024;
struct Params { const u16 *x, *wqkv, *wo, *wg, *wu, *wd; u16* out; char* ws; };

DI void mfma_acc(f32x4& c, u32x4 a, u32x4 b) {
  asm volatile("v_mfma_f32_16x16x32_bf16 %0, %1, %2, %0" : "+v"(c) : "v"(a), "v"(b));
}
DI void mfma_init(f32x4& c, u32x4 a, u32x4 b) {
  asm volatile("v_mfma_f32_16x16x32_bf16 %0, %1, %2, 0" : "=&v"(c) : "v"(a), "v"(b));
}
template <bool FIRST> DI void mfma_st(f32x4& c, u32x4 a, u32x4 b) { if constexpr (FIRST) mfma_init(c, a, b); else mfma_acc(c, a, b); }
DI float wave_sum(float v) {
  f32x4 a = mfmaf(v, 1.f);
  return mfmaf(1.f, (a[0] + a[1]) + (a[2] + a[3]))[0];
}
struct LnRow {
  float v[8], x0, s1, s2, mb, inv;
  DI void a(u32x4 x) {
#pragma unroll
    for (int e = 0; e < 4; ++e) { v[2 * e] = lo2f(x[e]); v[2 * e + 1] = hi2f(x[e]); }
    x0 = __uint_as_float(__builtin_amdgcn_readfirstlane(__float_as_uint(v[0])));
    s1 = 0; s2 = 0;
#pragma unroll
    for (int e = 0; e < 8; ++e) { float d = v[e] - x0; s1 += d; s2 += d * d; }
  }
  DI void b() {
    float dm = wave_sum(s1) * (1.f / 512.f);
    float var = relu(wave_sum(s2) * (1.f / 512.f) - dm * dm);
    mb = rbf(x0 + dm);
    float sd = __builtin_amdgcn_sqrtf(rbf(rbf(var) + 1e-5f));
    inv = __builtin_amdgcn_rcpf(lo2f(pk2(sd, sd)));
  }
  DI u32x4 c() {
    u32x4 o;
#pragma unroll
    for (int e = 0; e < 4; ++e) o[e] = cvtpk(rbf(v[2 * e] - mb) * inv, rbf(v[2 * e + 1] - mb) * inv);
    return o;
  }
};
DI void dma16_sc1_32lanes(unsigned lds_dst, unsigned voff, const void* sbase) {
  asm volatile("s_mov_b32 m0, %0\n\ts_mov_b64 exec, 0xffffffff\n\ts_nop 2\n\tglobal_load_lds_dwordx4 %1, %2 sc1\n\ts_mov_b64 exec, -1" :: "s"(lds_dst), "v"(voff), "s"(sbase) : "memory");
}
DI void poll_flags(GAS u64* f, u64 tag, unsigned need, int li) {
  const bool skip = !((need >> li) & 1);
#pragma unroll 1
  for (unsigned spins = 0; spins < (1u << 20); ++spins) {
    u64 v = __hip_atomic_load(f + li, __ATOMIC_RELAXED, __HIP_MEMORY_SCOPE_AGENT);
    if (__all(skip || v == tag)) break;
    __builtin_amdgcn_s_sleep(2);
  }
}

#define FHS 18
#define F0F 1
#define QPOLL 5
template <bool ISQ> struct Geo {
  static constexpr int NUNIT = ISQ ? 24 : 16, NSTEP = ISQ ? 32 : 16, A_SLOT = ISQ ? 18432 : 16384, B_SLOT = 20480;
  static constexpr int A_OFF = 102400, NT1 = ISQ ? 2 : 5;
  static constexpr int FLAG0_B = ISQ ? 1 : F0F, NPRO = ISQ ? 9 : 7, NPRO0 = ISQ ? 6 : 4, POLL_B = ISQ ? QPOLL : 11, FLAGH_B = 8 + FHS - 16;
  static constexpr int LASTRAW_B = ISQ ? 7 : 15;
  static constexpr int unit(int u) { return !ISQ ? u : (u < 16 ? u >> 1 : u - 8); }
  static constexpr int barrier_of(int u) { return !ISQ ? u : (u < 16 ? ((u & 1) ? u >> 1 : -1) : u - 8); }
  static constexpr int bslot(int k) { return (k + 5 * 9 + 4 - (NUNIT - 1)) % 5; }
  static constexpr int ntile(int u) { return !ISQ ? 5 : (u < 16 ? 2 : 3); }
  static constexpr int astage(int k) { return !ISQ ? k >> 1 : (k < 8 ? k : 8 + ((k - 8) >> 1)); }
};
struct Sched { int n; int op[8]; };
template <bool ISQ> constexpr Sched sched(int b) {
  using G = Geo<ISQ>;
  Sched r{0, {0, 0, 0, 0, 0, 0, 0, 0}};
  auto add = [&](int o) { r.op[r.n++] = o; };
  if (!ISQ) {
    if (b == G::POLL_B) add(401);
    if (b == 0) { add(103); add(104); }
    else if (b == 1) { add(304); add(305); add(105); }
    else if (b == 2) { add(106); add(306); add(307); }
    else if (b == 3) { add(107); add(108); }
    else if (b == 4) { add(109); add(308); add(309); }
    else if (b <= 10) { add(100 + b + 5); add(310 + (b - 5)); }
  } else {
    if (b <= 4) { add(306 + 2 * b); add(307 + 2 * b); }
    if (b == 0) { add(103); add(104); }
    if (b + 5 < G::NUNIT) add(100 + b + 5);
    if (b == G::POLL_B) add(401);
    for (int s = 8; s <= 10; ++s) if (b == (s - 3 > G::POLL_B ? s - 3 : G::POLL_B)) { add(300 + 2 * s); add(301 + 2 * s); }
    if (b >= 9 && b <= 18) add(322 + (b - 9));
  }
  if (b == G::NUNIT - 4) add(410);
  if (b == G::NUNIT - 2) add(411);
  if (b == G::NUNIT - 1) add(412);
  return r;
}
template <bool ISQ> constexpr int opcount(int o) {
  if (o >= 100 && o < 200) return !ISQ ? 5 : (o < 108 ? 4 : 3);
  if (o >= 300 && o < 400) return (o >= 316 && !(o & 1)) ? 3 : 2;
  if (o == 401) return 1;
  if (o >= 410 && o <= 412) return o == 412 ? 4 : 8;
  return 0;
}
constexpr int PROF[9] = {300, 301, 100, 101, 302, 303, 102, 0, 0}, PROQ[9] = {300, 301, 100, 302, 303, 101, 304, 305, 102};
template <bool ISQ> constexpr int pro(int i) { return ISQ ? PROQ[i] : PROF[i]; }
template <bool ISQ> constexpr int younger(int o, int b) {
  int n = 0; bool seen = false;
  for (int i = 0; i < Geo<ISQ>::NPRO; ++i) { if (seen) n += opcount<ISQ>(pro<ISQ>(i)); if (pro<ISQ>(i) == o) seen = true; }
  for (int v = 0; v < b; ++v) { Sched s = sched<ISQ>(v); for (int i = 0; i < s.n; ++i) { if (seen) n += opcount<ISQ>(s.op[i]); if (s.op[i] == o) seen = true; } }
  return seen ? n : -1000;
}
constexpr int imin(int a, int b) { return a < b ? a : b; }
template <bool ISQ> constexpr int allowed(int b) {
  using G = Geo<ISQ>;
  int n = 63;
  if (b + 1 < G::NUNIT) {
    const int s = G::astage(b + 1);
    n = imin(n, younger<ISQ>(100 + b + 1, b)); n = imin(n, younger<ISQ>(300 + 2 * s, b)); n = imin(n, younger<ISQ>(301 + 2 * s, b));
  }
  if (b == G::LASTRAW_B) n = imin(n, younger<ISQ>(401, b));
  return n;
}
template <bool ISQ> DI void b_bases(unsigned (&boff)[2][3], int lane, int wn) {
  using G = Geo<ISQ>;
  const int g = lane >> 4, q = (lane >> 2) & 3, pp = lane & 3, s = ((q >> 1) & 1) | ((g & 1) << 1);
  const unsigned rb = g * 1024 + 128 * q + 8 * pp;
  if (ISQ) boff[0][0] = rb + 32 * (wn ^ s);
  else {
    int p0 = wn >> 1, j0 = 2 * (wn & 1);
    boff[0][0] = rb + p0 * 4096 + 32 * (j0 ^ s); boff[0][1] = rb + p0 * 4096 + 32 * ((j0 + 1) ^ s);
    int up = pp >> 1, d = 2 * (g >> 1) + up, r16 = 8 * (g & 1) + q, sg = (g & 1) | (up << 1);
    boff[0][2] = 16384 + d * 1024 + 64 * r16 + 16 * (wn ^ sg) + 8 * (pp & 1);
  }
#pragma unroll
  for (int c = 0; c < (ISQ ? 1 : 3); ++c) { boff[1][c] = boff[0][c] + 3 * G::B_SLOT; asm volatile("" : "+v"(boff[0][c]), "+v"(boff[1][c])); }
}
template <bool ISQ, int U, int C> DI u32x4 rd_b(const char* lds, const unsigned (&boff)[2][3]) {
  using G = Geo<ISQ>;
  constexpr int sl = G::bslot(G::unit(U)), hb = sl >= 3, bo = (sl - 3 * hb) * G::B_SLOT;
  if constexpr (ISQ) return lds_tr2(lds, boff[hb][0] + bo + ((U < 16 ? 2 * (U & 1) : 0) + C) * 4096, 512);
  else if constexpr (C < 4) return lds_tr2(lds, boff[hb][C & 1] + bo + (C >> 1) * 8192, 512);
  else return lds_tr2(lds, boff[hb][2] + bo, 256);
}
#define VF 4
#define VPUBQ 2
#define VO 24
constexpr int p2A(int i) { if (i < NA2) return 0; int v = (i - NA2) / 2 + 2, gt = i < 56 ? VF : VO; return v > gt ? v : gt; }
constexpr int p2W(int i) { return i < NW2 ? 0 : (i - NW2) / 2 + 2; }
constexpr int p2ops(int v) { int n = 0; for (int i = 0; i < 72; ++i) n += (p2A(i) == v) + (p2W(i) == v); return n; }
constexpr int p2allowed(int v) {
  int nA = 0, nW = 0; bool sa = false, sw = false;
  for (int pp = 0; pp < (v ? v : 1); ++pp) {
    if (pp == 0) for (int i = 0; i < 72; ++i) if (p2W(i) == 0) { if (sa) ++nA; if (sw) ++nW; if (i == 2 * v + 1) sw = true; }
    for (int i = 0; i < 72; ++i) if (p2A(i) == pp) { if (sa) ++nA; if (sw) ++nW; if (i == 2 * v + 1) sa = true; }
    if (pp > 0) for (int i = 0; i < 72; ++i) if (p2W(i) == pp) { if (sa) ++nA; if (sw) ++nW; if (i == 2 * v + 1) sw = true; }
  }
  return (sa && sw) ? (nA < nW ? nA : nW) : -1;
}
template <int I> DI int p2phys(int r8) {
  if constexpr (I < 16) return (I + 2 * r8) & 15;
  else if constexpr (I < 56) { int t = I - 16 + 5 * r8; return 16 + (t >= 40 ? t - 40 : t); }
  else return 56 + ((I - 56 + 2 * r8) & 15);
}

template <bool ISQ>
DI void loader1(const Params& p, char* lds, int mt, int j, int rot, int nt, GAS u64* sync, u64& tag) {
  using G = Geo<ISQ>;
  const int lane = threadIdx.x & 63, l = __builtin_amdgcn_readfirstlane(threadIdx.x >> 6) - 8, li = lane & 15, g = lane >> 4;
  const unsigned lbase = (unsigned)(size_t)lds;
  const int T0 = mt * 128;
  char* xn = p.ws + mt * GRP + XN_OFF;
  GAS u64* flags0 = sync + 128; GAS u64* flagsH = sync + 384;
  __builtin_amdgcn_s_setprio(3);
  int fa = 0, fx = 0;
  if (!ISQ) { int i = j - 8, f0 = 512 + 160 * i; if (i & 1) { fx = f0; fa = f0 + 32; } else { fa = f0; fx = f0 + 128; } }
  const int q = lane >> 3, slot = lane & 7;
  const unsigned co = 16 * (2 * ((slot >> 1) ^ (((q >> 1) & 1) | ((l & 1) << 1))) + (slot & 1));
  const unsigned voF = (8 * l + q) * 3584 + co, voQ = (8 * l + q) * 3072 + co;
  unsigned voC = 0;
  if (!ISQ) { int r16 = lane >> 2, cp = lane & 3, sg = ((r16 >> 3) & 1) | ((l & 1) << 1); voC = (16 * (l >> 1) + r16) * 3584 + 16 * (cp ^ sg); }
  const char* wgb = (const char*)(p.wg + (ISQ ? 64 * j : fa));
  const char* wub = (const char*)(p.wu + (ISQ ? 64 * j : fa));
  const char* wcb = ISQ ? (const char*)(p.wqkv + 64 * j) : (const char*)(((l & 1) ? p.wu : p.wg) + fx);
  const unsigned c0 = 16 * (slot ^ (q >> 1)), c1 = 16 * (slot ^ (4 + (q >> 1)));
  const unsigned vr0 = (unsigned)(T0 + 32 * l + 2 * q) * 1024u + c0, vr1 = (unsigned)(T0 + 32 * l + 16 + 2 * q) * 1024u + c1;
  const unsigned vx0 = (32 * l + q) * XP + c0, vx1 = (32 * l + 8 + q) * XP + c1;
  unsigned vxh; { int rho = 128 + 4 * l + (q & 3); vxh = rho * XP + 16 * (slot ^ ((rho >> 1) & 7)); }
  const unsigned wvo = q * 1024 + 16 * (2 * ((slot >> 1) ^ ((q >> 1) & 3)) + (slot & 1));
  auto doop = [&](auto oc) {
    constexpr int o = oc;
    if constexpr (o >= 100 && o < 200) {
      constexpr int u = o - 100;
      const unsigned dst = lbase + G::bslot(u) * G::B_SLOT + l * 1024, kr = 32 * (((ISQ ? (u < 8 ? 2 * u : u - 8) : u) + rot) & 15);
      if constexpr (ISQ) {
        if constexpr (u >= 8) { const char* b = wcb + kr * 3072; dma16(dst, voQ, b); dma16(dst + 4096, voQ, b + 1024); dma16(dst + 8192, voQ, b + 2048); }
        else {
          const char *b0 = wgb + kr * 3584, *b1 = wub + kr * 3584;
          dma16(dst, voF, b0); dma16(dst + 4096, voF, b1); dma16(dst + 8192, voF, b0 + 32 * 3584); dma16(dst + 12288, voF, b1 + 32 * 3584);
        }
      } else {
        const char *b0 = wgb + kr * 3584, *b1 = wub + kr * 3584;
        dma16(dst, voF, b0); dma16(dst + 4096, voF, b0 + 128); dma16(dst + 8192, voF, b1); dma16(dst + 12288, voF, b1 + 128);
        dma16(dst + 16384, voC, wcb + kr * 3584);
      }
    } else if constexpr (o >= 300 && o < 400) {
      constexpr int s = (o - 300) >> 1, h = o & 1;
      const unsigned dst = lbase + G::A_OFF + (s % 3) * G::A_SLOT, ko = 64 * ((2 * s + rot) & 15);
      if constexpr (s < 8) {
        const char* b = (const char*)p.x + ko + h * 1024;
        dma16(dst + (4 * l + 2 * h) * 1024, vr0, b); dma16(dst + (4 * l + 2 * h + 1) * 1024, vr1, b);
      } else {
        const char* b = xn + ko + h * 16 * XP;
        dma16_sc1(dst + (4 * l + 2 * h) * 1024, vx0, b); dma16_sc1(dst + (4 * l + 2 * h + 1) * 1024, vx1, b);
        if constexpr (h == 0) dma16_sc1_32lanes(dst + 16384 + l * 512, vxh, xn + ko);
      }
    } else if constexpr (o == 401) {
      poll_flags(flags0 + j * 16, tag, 0xffffu, li);
      dma16_sc1(lbase + SL_OFF, lane * 16, p.ws + mt * GRP + ST_OFF);
    } else if constexpr (o >= 410 && o <= 412) {
      static_for<(o == 412 ? 4 : 8)>([&](auto kc) {
        constexpr int st = 8 * (o - 410) + kc;
        dma16(lbase + st * 4096 + l * 1024, wvo, (const char*)p.wd + (size_t)p2phys<st>(rot >> 1) * 32768 + l * 8192 + nt * 128);
      });
    }
  };
  static_for<G::NPRO0>([&](auto i) { doop(IC<pro<ISQ>(i)>{}); });
  wait_vm<(ISQ ? 8 : 5)>();
  if (l == 0 && lane < 4) *(unsigned*)(lds + RM_OFF + lane * 4) = 0;
  unsigned boff[2][3];
  b_bases<ISQ>(boff, lane, l);
  u32x4 ones = {0x3f803f80u, 0x3f803f80u, 0x3f803f80u, 0x3f803f80u};
  f32x4 cs[G::NT1];
#pragma unroll
  for (int c = 0; c < G::NT1; ++c) cs[c] = f32x4{0, 0, 0, 0};
  auto colsum = [&](auto uc) {
    u32x4 b[G::NT1];
    static_for<G::NT1>([&](auto c) { b[c] = rd_b<ISQ, uc, c>(lds, boff); });
#pragma unroll
    for (int c = 0; c < G::NT1; ++c) cs[c] = mfma32(b[c], ones, cs[c]);
  };
  wait_lgkm0();
  bar();
  static_for<G::NPRO - G::NPRO0>([&](auto i) { doop(IC<pro<ISQ>(G::NPRO0 + i)>{}); });
  { const u64 t = *(const u64*)(lds + RM_OFF + 16);
    tag = ((u64)(unsigned)__builtin_amdgcn_readfirstlane((unsigned)(t >> 32)) << 32) | (unsigned)__builtin_amdgcn_readfirstlane((unsigned)t); }
  colsum(IC<0>{});
  if constexpr (ISQ) colsum(IC<1>{});
  static_for<G::NUNIT>([&](auto uc) {
    constexpr int u = uc;
    constexpr Sched S = sched<ISQ>(u);
    static_assert(allowed<ISQ>(u) >= 0, "schedule");
    wait_vm<allowed<ISQ>(u)>();
    wait_lgkm0();
    bar();
    if constexpr (u == G::FLAG0_B) { if (l == 3 && lane < 16) __hip_atomic_store(flags0 + lane * 16 + j, tag, __ATOMIC_RELAXED, __HIP_MEMORY_SCOPE_AGENT); }
    if constexpr (ISQ && u == G::FLAGH_B) { if (l == 3 && lane < 16) __hip_atomic_store(flagsH + lane * 16 + j, tag, __ATOMIC_RELAXED, __HIP_MEMORY_SCOPE_AGENT); }
    static_for<S.n>([&](auto i) { doop(IC<S.op[i]>{}); });
    if constexpr (u < G::LASTRAW_B) { if constexpr (ISQ) { colsum(IC<2 * u + 2>{}); colsum(IC<2 * u + 3>{}); } else colsum(IC<u + 1>{}); }
    if constexpr (u == G::LASTRAW_B - 1) {
      if (li == 0) {
#pragma unroll
        for (int c = 0; c < G::NT1; ++c) *(f32x4*)(lds + CS_OFF + (l * 5 + c) * 64 + g * 16) = cs[c];
      }
    }
  });
}

template <bool ISQ>
DI void compute1(const Params& p, char* lds, int mt, int j, const char* code, int code_kb) {
  using G = Geo<ISQ>;
  constexpr int NSTEP = G::NSTEP, NT1 = G::NT1, A_SLOT = G::A_SLOT, A_OFF = G::A_OFF;
  const int tid = threadIdx.x, lane = tid & 63;
  const int w = __builtin_amdgcn_readfirstlane(tid >> 6), wm = w >> 2, wn = w & 3;
  const int li = lane & 15, g = lane >> 4;
  const int T0 = mt * 128;
  const unsigned lo16 = lane * 16;
  {
    u32x4 xr, xh;
    int rho = 8 * j + w; int tok = T0 + 32 * (rho >> 5) + 2 * (rho & 15) + ((rho >> 4) & 1);
    gload<0>(xr, lo16, (const char*)p.x + (size_t)tok * 1024);
    gload<0>(xh, lo16, (const char*)p.x + (size_t)(w == 0 ? (mt ? T0 - 16 : 0) + 2 * (j & 7) + (j >> 3) : tok) * 1024);
    u64 ep;
    gload8_sc1(ep, 0u, p.ws + mt * GRP + SYNC_OFF);
#pragma unroll
    for (int k = 0; k < 4; ++k) {
      int c = (k * 256 + (int)blockIdx.x) * 8 + w;
      const char* src = (const char*)p.wg;
      if (c >= 1792) { src = (const char*)p.wu; c -= 1792;
        if (c >= 1792) { src = (const char*)p.wqkv; c -= 1792;
          if (c >= 1536) { src = (const char*)p.wd; c -= 1536;
            if (c >= 1792) { src = (const char*)p.wo; c -= 1792;
              if (c >= 512) { src = code; c = (((int)blockIdx.x - 160) >> 3) * 8 + w; c = c < code_kb ? c : 0; } } } } }
      dma16((unsigned)(size_t)lds + WU_OFF, lo16, src + (size_t)c * 1024);
    }
    wait_vm<4>();
    asm volatile("" : "+v"(xr), "+v"(xh), "+v"(ep));
    *(u64*)(lds + RM_OFF + 16) = ep + 1;
    LnRow r; r.a(xr); r.b();
    gstore_sc1(r.c(), lo16, p.ws + mt * GRP + XN_OFF + rho * XP);
    f32x2 st = {r.mb, r.inv};
    asm volatile(HZ "global_store_dwordx2 %0, %1, %2 sc1\n\ts_nop 1" :: "v"(0u), "v"(st), "s"(p.ws + mt * GRP + ST_OFF + rho * 8) : "memory");
    if (w == 0) {
      LnRow r2; r2.a(xh); r2.b();
      gstore_sc1(r2.c(), lo16, p.ws + mt * GRP + XN_OFF + (128 + j) * XP);
    }
  }
  int fa = 0, fx = 0;
  if (!ISQ) { int i = j - 8, f0 = 512 + 160 * i; if (i & 1) { fx = f0; fa = f0 + 32; } else { fa = f0; fx = f0 + 128; } }
  unsigned aoff[2], hoff[2], boff[2][3];
  { int rho = 64 * wm + li; int f = (rho >> 1) & 7; aoff[0] = A_OFF + rho * 128 + 16 * (g ^ f); aoff[1] = A_OFF + rho * 128 + 16 * ((4 + g) ^ f); }
  b_bases<ISQ>(boff, lane, wn);
  asm volatile("" : "+v"(aoff[0]), "+v"(aoff[1]));
  if (ISQ) {
    int rho = wm ? (40 + (li & 7) + 16 * (li >> 3)) : 128 + li; int f = (rho >> 1) & 7;
    hoff[0] = A_OFF + rho * 128 + 16 * (g ^ f); hoff[1] = A_OFF + rho * 128 + 16 * ((4 + g) ^ f);
    asm volatile("" : "+v"(hoff[0]), "+v"(hoff[1]));
  }
  f32x4 acc[4][NT1];
  f32x4 aq[4][3], acch[2];
  u32x4 af[2][4], ah[2], bf[4], bl[2];
  char* a2b = p.ws + mt * GRP + (size_t)(2 * wm) * NK16 * 1024;
  f32x4 cs[NT1]; f32x2 stt[4];
  auto get_tables = [&]() {
#pragma unroll
    for (int c = 0; c < NT1; ++c) cs[c] = *(const f32x4*)(lds + CS_OFF + (wn * 5 + c) * 64 + g * 16);
#pragma unroll
    for (int r = 0; r < 4; ++r) stt[r] = *(const f32x2*)(lds + SL_OFF + (64 * wm + 16 * r + li) * 8);
  };
  auto reglu = [&](f32x4 ga, f32x4 cg, f32x4 ua, f32x4 cu, f32x2 st) {
    float i2 = st[1] * st[1]; float h[4];
#pragma unroll
    for (int e = 0; e < 4; ++e) h[e] = relu(ga[e] - st[0] * cg[e]) * ((ua[e] - st[0] * cu[e]) * i2);
    u32x2 r; r[0] = cvtpk(h[0], h[1]); r[1] = cvtpk(h[2], h[3]); return r;
  };
  auto rdA = [&](auto uc, auto rc) {
    constexpr int u = uc, r = rc, P = u & 1, ao = ((u >> 1) % 3) * A_SLOT;
    if constexpr (r < 4) af[P][r] = lds_b128(lds, aoff[P] + ao + r * 2048);
    else ah[P] = lds_b128(lds, hoff[P] + ao);
  };
  wait_lgkm0();
  bar();
  static_for<4>([&](auto r) { rdA(IC<0>{}, r); });
  static_for<NT1 - 1>([&](auto c) { bf[c] = rd_b<ISQ, 0, c>(lds, boff); });
  bl[0] = rd_b<ISQ, 0, NT1 - 1>(lds, boff);
  static_for<NSTEP>([&](auto uc) {
    constexpr int u = uc; constexpr int P = u & 1;
    constexpr bool XQ = ISQ && u >= 16, NXQ = ISQ && u + 1 >= 16, MORE = u + 1 < NSTEP;
    constexpr int BI = G::barrier_of(u);
    constexpr int NC = G::ntile(u), NCN = G::ntile(MORE ? u + 1 : u);
    static_for<NC>([&](auto cc) {
      constexpr int c = cc;
      constexpr bool QKV = XQ && c < 3;
      constexpr int NR = 4 + ((QKV && c >= 1) ? 1 : 0);
      static_for<NR>([&](auto rc) {
        constexpr int r = rc;
        const u32x4 b = (c == NC - 1) ? bl[P] : bf[c < 4 ? c : 0];
        if constexpr (QKV) {
          if constexpr (r < 4) { if constexpr (c == 2) mfma_st<u == 16>(aq[r][c], af[P][r], b); else mfma_st<u == 16>(aq[r][c], b, af[P][r]); }
          else if constexpr (c == 1) mfma_st<u == 16>(acch[0], b, ah[P]);
          else mfma_st<u == 16>(acch[1], ah[P], b);
        } else mfma_st<u == 0>(acc[r][XQ ? c - 3 : c], b, af[P][r]);
        SCHED;
        if constexpr (c == 0 && r == 1 && BI >= 0) {
          if constexpr (BI == G::FLAG0_B) wait_vm<0>();
          if constexpr (ISQ && BI == G::FLAGH_B) wait_vm<0>();
          wait_lgkm0();
          bar();
        }
        if constexpr (MORE) {
          if constexpr (c == 0 && r == 2) { rdA(IC<u + 1>{}, IC<0>{}); rdA(IC<u + 1>{}, IC<1>{}); }
          if constexpr (c == 1 && r == 0) rdA(IC<u + 1>{}, IC<2>{});
          if constexpr (c == 1 && r == 1) rdA(IC<u + 1>{}, IC<3>{});
          if constexpr (c == 1 && r == 2) bl[1 - P] = rd_b<ISQ, u + 1, NCN - 1>(lds, boff);
          if constexpr (c == 1 && r == 2 && NXQ) rdA(IC<u + 1>{}, IC<4>{});
          if constexpr (r == NR - 1 && c < NC - 1 && c < NCN - 1) bf[c] = rd_b<ISQ, u + 1, c>(lds, boff);
          if constexpr (c == 1 && r < 3 && NC - 1 + r < NCN - 1) bf[NC - 1 + r] = rd_b<ISQ, u + 1, NC - 1 + r>(lds, boff);
        }
        SCHED;
      });
    });
    if constexpr (ISQ && u == 15) {
      get_tables();
      asm volatile("s_nop 15\n\ts_nop 7" ::: "memory");
      SCHED;
#pragma unroll
      for (int k = 0; k < 2; ++k) {
        u32x2 h0 = reglu(acc[2 * k][0], cs[0], acc[2 * k][1], cs[1], stt[2 * k]), h1 = reglu(acc[2 * k + 1][0], cs[0], acc[2 * k + 1][1], cs[1], stt[2 * k + 1]);
        gstore_sc1(cat(h0, h1), lo16, a2b + (size_t)(32 + 4 * j + wn + k * NK16) * 1024);
      }
      SCHED;
    }
  });
  asm volatile("s_nop 15\n\ts_nop 7" ::: "memory");
  SCHED;
  if constexpr (ISQ) {
    f32x4 bprev, bh[2]; bool cv[4];
    {
      const float NEG = -1e30f;
      const bool nohalo = (mt == 0) && (wm == 0);
      int ln = lane; asm volatile("" : "+v"(ln));
      const int d0 = (ln & 15) - 4 * (ln >> 4);
#pragma unroll
      for (int e = 0; e < 4; ++e) {
        int d = d0 - e;
        cv[e] = (unsigned)d <= 7u;
        bprev[e] = (d <= -9) ? 0.f : NEG;
        bh[0][e] = (d < 0 && ln < 32 && !nohalo) ? 0.f : NEG;
        bh[1][e] = nohalo ? NEG : bprev[e];
      }
    }
    u32x2 Qp[4], Kp[4], Vp[4], Kh, Vh;
#pragma unroll
    for (int r = 0; r < 4; ++r) { Qp[r] = pk4(aq[r][0]); Kp[r] = pk4(aq[r][1]); Vp[r] = pk4(aq[r][2]); }
    Kh = pk4(acch[0]); Vh = pk4(acch[1]);
    const float SC = 0.25f * 1.44269504088896f;
    const unsigned mc0 = (cv[0] ? 0xffffu : 0u) | (cv[1] ? 0xffff0000u : 0u);
    const unsigned mc1 = (cv[2] ? 0xffffu : 0u) | (cv[3] ? 0xffff0000u : 0u);
    float l[4][4], mx[4];
#pragma unroll
    for (int r = 0; r < 4; ++r) {
      f32x4 sc = mfma16(Kp[r], Qp[r], f32x4{0, 0, 0, 0});
      f32x4 sp = (r >= 2) ? mfma16(Kp[r - 2], Qp[r], bprev) : mfma16(Kh, Qp[r], bh[r & 1]);
#pragma unroll
      for (int e = 0; e < 4; ++e) l[r][e] = rbf(cv[e] ? sc[e] : sp[e]);
      mx[r] = vmax(vmax(l[r][0], l[r][1]), vmax(l[r][2], l[r][3]));
    }
    u32x2 ov[4];
#pragma unroll
    for (int r = 0; r < 4; ++r) mx[r] = gmax(mx[r]) * SC;
#pragma unroll
    for (int r = 0; r < 4; ++r) {
#pragma unroll
      for (int e = 0; e < 4; ++e) l[r][e] = __builtin_amdgcn_exp2f(l[r][e] * SC - mx[r]);
      float sum = mfmaf(1.f, (l[r][0] + l[r][1]) + (l[r][2] + l[r][3]))[0];
      float rs = __builtin_amdgcn_rcpf(sum);
      unsigned p0 = cvtpk(l[r][0] * rs, l[r][1] * rs), p1 = cvtpk(l[r][2] * rs, l[r][3] * rs);
      u32x4 pb; pb[0] = p0 & ~mc0; pb[1] = p1 & ~mc1; pb[2] = p0 & mc0; pb[3] = p1 & mc1;
      f32x4 o = mfma32(cat((r >= 2) ? Vp[r - 2] : Vh, Vp[r]), pb, f32x4{0, 0, 0, 0});
      ov[r] = pk4(o);
    }
    char* pb = a2b + (size_t)(4 * j + wn) * 1024;
    gstore_sc1(cat(ov[0], ov[1]), lo16, pb);
    gstore_sc1(cat(ov[2], ov[3]), lo16, pb + NK16 * 1024);
  } else {
    get_tables();
    char* pb = a2b + (size_t)(32 + (fa >> 4) + 2 * wn) * 1024;
#pragma unroll
    for (int c = 0; c < 2; ++c) {
      u32x2 hv[4];
#pragma unroll
      for (int r = 0; r < 4; ++r) hv[r] = reglu(acc[r][c], cs[c], acc[r][2 + c], cs[2 + c], stt[r]);
      gstore_sc1(cat(hv[0], hv[1]), lo16, pb + c * 1024);
      gstore_sc1(cat(hv[2], hv[3]), lo16, pb + c * 1024 + NK16 * 1024);
    }
    u32x2 hq[2];
#pragma unroll
    for (int rp = 0; rp < 2; ++rp) {
      float h[4];
#pragma unroll
      for (int e = 0; e < 4; ++e) {
        float a = (acc[rp][4][e] - stt[rp][0] * cs[4][e]) * stt[rp][1], b = (acc[rp + 2][4][e] - stt[rp + 2][0] * cs[4][e]) * stt[rp + 2][1];
        auto r = __builtin_amdgcn_permlane32_swap(__float_as_uint(a), __float_as_uint(b), false, false);
        h[e] = relu(__uint_as_float(r[0])) * __uint_as_float(r[1]);
      }
      hq[rp][0] = cvtpk(h[0], h[1]); hq[rp][1] = cvtpk(h[2], h[3]);
    }
    unsigned vo = (unsigned)((g >> 1) * NK16 * 1024 + ((2 * (wn & 1) + (g & 1)) * 16 + li) * 16);
    gstore_sc1(cat(hq[0], hq[1]), vo, a2b + (size_t)(32 + ((fx + 8 * wn) >> 4)) * 1024);
  }
}

__global__ __launch_bounds__(768, 3) void fused(Params p) {
  __shared__ __attribute__((aligned(16))) char lds[LDS_BYTES];
  const int tid = threadIdx.x, lane = tid & 63, w = __builtin_amdgcn_readfirstlane(tid >> 6);
  const int li = lane & 15, g = lane >> 4;
  const int bid = blockIdx.x, xcd = bid & 7, idx = bid >> 3;
  const int mt = (xcd & 3) * 4 + (idx >> 3), s8 = idx & 7, nh = xcd >> 2;
  const int nt = s8 ^ 5;
  const unsigned lbase = (unsigned)(size_t)lds;
  GAS u64* sync = (GAS u64*)(p.ws + mt * GRP + SYNC_OFF);
  GAS u64* flagsH = sync + 384; GAS u64* flagsO = sync + 640;
  u64 pc; int beg, len;
  asm volatile("s_getpc_b64 %0\n.Lfused_pf_here:\n\ts_mov_b32 %1, .Lfused_pf_here-_Z5fused6Params\n\ts_mov_b32 %2, .Lfused_pf_end-_Z5fused6Params" : "=s"(pc), "=s"(beg), "=s"(len));
  if (w < 8) { int c = idx * 8 + w; c = c - (c / 72) * 72; c = c < (len >> 10) ? c : 0;
    dma16(lbase + WU_OFF, lane * 16, (const char*)(pc - (u64)beg) + (size_t)c * 1024); }
  const int j = s8 < 4 ? 4 * nh + s8 : 8 + 4 * nh + (s8 - 4);
  if (w >= 8) {
    u64 tag;
    const int rot = 2 * xcd;
    if (s8 < 4) loader1<true>(p, lds, mt, j, rot, nt, sync, tag);
    else loader1<false>(p, lds, mt, j, rot, nt, sync, tag);
    {
      const int l = w - 8;
      unsigned wvo;
      { int q = lane >> 3, slot = lane & 7; wvo = q * 1024 + 16 * (2 * ((slot >> 1) ^ ((q >> 1) & 3)) + (slot & 1)); }
      const char* ab = p.ws + mt * GRP + ((size_t)(2 * nh + (l >> 1)) * NK16 + (l & 1)) * 1024;
      const char* wdb = (const char*)p.wd + l * 8192 + nt * 128;
      const char* wob = (const char*)p.wo + l * 8192 + nt * 128;
      const unsigned lo16 = lane * 16;
      u64 look = 0;
      auto issW = [&](auto ic) { constexpr int i = ic;
        dma16(lbase + (i % NW2) * 4096 + l * 1024, wvo, i < 56 ? wdb + (size_t)p2phys<i>(xcd) * 32768 : wob + (size_t)(p2phys<i>(xcd) - 56) * 32768); };
      auto issA = [&](auto ic) { constexpr int i = ic;
        if constexpr (i == 0) poll_flags(flagsH + j * 16, tag, 0x00ffu, li);
        if constexpr (i == 16) {
          wait_vm<p2ops(VF - 2) + p2ops(VF - 1)>();
          pin64(look);
          if (!__all(li < 8 || look == tag)) poll_flags(flagsH + j * 16, tag, 0xff00u, li);
        }
        if constexpr (i == 56) {
          wait_vm<p2ops(VO - 2) + p2ops(VO - 1)>();
          pin64(look);
          if (!__all(li >= 8 || look == tag)) poll_flags(flagsO + j * 16, tag, 0x00ffu, li);
        }
        dma16_sc1(lbase + A2_OFF + (i % NA2) * 4096 + l * 1024, lo16, ab + (size_t)(i < 56 ? 16 + p2phys<i>(xcd) : p2phys<i>(xcd) - 56) * 2048); };
      static_for<NA2>([&](auto i) { issA(i); });
      static_for<36>([&](auto vc) {
        constexpr int v = vc;
        static_assert(p2allowed(v) >= 0 && p2allowed(v) < 64, "schedule");
        wait_vm<p2allowed(v)>();
        bar();
        if constexpr (v >= 1) {
          static_for<72>([&](auto i) { if constexpr (p2A(i) == v) issA(i); });
          static_for<72>([&](auto i) { if constexpr (p2W(i) == v) issW(i); });
        }
        if constexpr (v == VF - 3) gload8_sc1(look, (unsigned)(li * 8), (const void*)(flagsH + j * 16));
        if constexpr (v == VO - 3) gload8_sc1(look, (unsigned)(li * 8), (const void*)(flagsO + j * 16));
      });
      bar();
      if (l == 0 && lane == 0 && j == 0) __hip_atomic_store(sync, tag, __ATOMIC_RELAXED, __HIP_MEMORY_SCOPE_AGENT);
    }
  } else {
  if (s8 < 4) compute1<true>(p, lds, mt, j, (const char*)(pc - (u64)beg), len >> 10);
  else compute1<false>(p, lds, mt, j, (const char*)(pc - (u64)beg), len >> 10);
  LAS unsigned* ctl = (LAS unsigned*)(lds + RM_OFF);
  const int kh = w >> 2, pr = (w >> 1) & 1, ch = w & 1;
  unsigned tw[2][2], ta[2];
  {
    int ln = lane; asm volatile("" : "+v"(ln));
    int q = (ln >> 2) & 3, pp = ln & 3, gg = ln >> 4, qq = 4 * (gg & 1) + q;
#pragma unroll
    for (int c = 0; c < 2; ++c) {
      tw[0][c] = kh * 4096 + (gg >> 1) * 1024 + 128 * qq + 32 * ((2 * ch + c) ^ ((qq >> 1) & 3)) + 8 * pp; tw[1][c] = tw[0][c] + 10 * 4096;
      asm volatile("" : "+v"(tw[0][c]), "+v"(tw[1][c]));
    }
    ta[0] = A2_OFF + kh * 4096 + pr * 2048 + ln * 16; ta[1] = ta[0] + 8 * 4096;
    asm volatile("" : "+v"(ta[0]), "+v"(ta[1]));
  }
  f32x4 ag2[2][2], as2[2][2];
  u32x2 xv[2];
  const int rho = 128 * mt + 64 * nh + 32 * pr + 16 * kh + li, tok = 32 * (rho >> 5) + 2 * (rho & 15) + ((rho >> 4) & 1);
  u32x4 fa2[2][2], fb2[2][2];
  float si[2][4];
  static_for<37>([&](auto vc) {
    constexpr int v = vc, P = v & 1, sw = (2 * v) % NW2, sa = (2 * v) % NA2;
    if constexpr (v == 0 || v == VPUBQ) if ((s8 < 4) == (v != 0)) {
      wait_vm<0>();
      unsigned old = 0;
      if (lane == 0) old = __hip_atomic_fetch_add(ctl + 1, 1u, __ATOMIC_RELAXED, __HIP_MEMORY_SCOPE_WORKGROUP);
      if (__builtin_amdgcn_readfirstlane(old) == 7) {
        const u64 tag = *(const u64*)(lds + RM_OFF + 16);
        if (lane < 16) asm volatile(HZ "global_store_dwordx2 %0, %1, %2 sc1\n\ts_nop 1" :: "v"((unsigned)((lane * 16 + j) * 8)), "v"(tag), "s"(s8 < 4 ? flagsO : flagsH) : "memory");
      }
    }
    if constexpr (v == 30) {
#pragma unroll
      for (int c = 0; c < 2; ++c)
        asm volatile(HZ "global_load_dwordx2 %0, %1, %2" : "=v"(xv[c]) : "v"((unsigned)(tok * 1024 + nt * 128 + (2 * ch + c) * 32 + g * 8)), "s"(p.x) : "memory");
    }
    if constexpr (v < 36) bar();
    u32x4 f0, f1;
    if constexpr (v < 36) fa2[P][0] = lds_b128(lds, ta[sa >= 8] + (sa & 7) * 4096);
    if constexpr (v >= 1) { const u32x4 a0 = fa2[1 - P][0], a1 = fa2[1 - P][1]; f0 = u32x4{a0[0], a0[1], a1[0], a1[1]}; f1 = u32x4{a0[2], a0[3], a1[2], a1[3]}; }
    SCHED;
    static_for<4>([&](auto mc) {
      constexpr int m = mc, r = m >> 1, c = m & 1;
      if constexpr (v < 36) {
        if constexpr (m == 0) fa2[P][1] = lds_b128(lds, ta[sa >= 8] + (sa & 7) * 4096 + 1024);
        if constexpr (m == 1 || m == 2) fb2[P][m - 1] = lds_tr2(lds, tw[sw >= 10][m - 1] + (sw % 10) * 4096, 2048);
      }
      if constexpr (v >= 1) {
        f32x4(&acc)[2][2] = v - 1 < 28 ? ag2 : as2;
        acc[r][c] = mfma32(fb2[1 - P][c], r ? f1 : f0, (v == 1 || v == 29) ? f32x4{0, 0, 0, 0} : acc[r][c]);
      }
      SCHED;
    });
    if constexpr (v == 30) {
#pragma unroll
      for (int c = 0; c < 2; ++c) *(f32x4*)(lds + 49152 + w * 2048 + c * 1024 + lane * 16) = kh ? ag2[0][c] : ag2[1][c];
      wait_lgkm0();
    }
    if constexpr (v == 32 || v == 33) {
      constexpr int c = v - 32;
      const f32x4 gs = (kh ? ag2[1][c] : ag2[0][c]) + *(const f32x4*)(lds + 49152 + (w ^ 4) * 2048 + c * 1024 + lane * 16);
#pragma unroll
      for (int e = 0; e < 4; ++e) {
        float d = rbf(gs[e]);
        si[c][e] = rbf(d * __builtin_amdgcn_rcpf(1.f + __builtin_amdgcn_exp2f(-1.44269504088896f * d)));
        asm volatile("" : "+v"(si[c][e]));
      }
    }
  });
  {
    char* xo = lds + w * 4096 + lane * 16;
#pragma unroll
    for (int c = 0; c < 2; ++c) *(f32x4*)(xo + 2048 + c * 1024) = kh ? as2[0][c] : as2[1][c];
  }
  wait_lgkm0();
  bar();
  wait_vm<0>();
  asm volatile("" : "+v"(xv[0]), "+v"(xv[1]));
  u32x2 o[2];
#pragma unroll
  for (int c = 0; c < 2; ++c) {
    const char* xi = lds + (w ^ 4) * 4096 + lane * 16;
    const f32x4 ssum_ = (kh ? as2[1][c] : as2[0][c]) + *(const f32x4*)(xi + 2048 + c * 1024);
    float ov[4];
#pragma unroll
    for (int e = 0; e < 4; ++e) {
      float xx = (e & 1) ? hi2f(xv[c][e >> 1]) : lo2f(xv[c][e >> 1]);
      ov[e] = xx + rbf(rbf(ssum_[e]) * si[c][e]);
    }
    o[c][0] = pk2(ov[0], ov[1]); o[c][1] = pk2(ov[2], ov[3]);
  }
  {
    auto r0 = __builtin_amdgcn_permlane16_swap(o[0][0], o[1][0], false, false);
    auto r1 = __builtin_amdgcn_permlane16_swap(o[0][1], o[1][1], false, false);
    const u32x4 ov4 = {r0[0], r1[0], r0[1], r1[1]};
    const unsigned oo = (unsigned)(tok * 1024 + nt * 128 + (2 * ch + (g & 1)) * 32 + (g >> 1) * 16);
    gstore_sc1(ov4, oo, p.out);
  }
  }
  asm volatile(".Lfused_pf_end:");
}

extern "C" void kernel_launch(void* const* d_in, const int* in_sizes, int n_in, void* d_out, int out_size, void* d_ws, size_t ws_size,
                              hipStream_t stream) {
  Params p;
  p.x = (const u16*)d_in[0]; p.wqkv = (const u16*)d_in[1]; p.wo = (const u16*)d_in[2];
  p.wg = (const u16*)d_in[4]; p.wu = (const u16*)d_in[5]; p.wd = (const u16*)d_in[6];
  p.out = (u16*)d_out; p.ws = (char*)d_ws;
  fused<<<256, 768, 0, stream>>>(p);
}
```
